```python
import torch

def _softmax(x, axis=-1):
    return torch.softmax(x, dim=axis)
def _gelu(x):
    return torch.nn.functional.gelu(x, approximate="tanh")
def _silu(x):
    return torch.nn.functional.silu(x)
def _sigmoid(x):
    return torch.sigmoid(x)
def _layernorm(x, axis=-1, eps=1e-5):
    m = x.mean(dim=axis, keepdim=True)
    v = x.var(dim=axis, keepdim=True, unbiased=False)
    return (x - m) / torch.sqrt(v + eps)
def _scatter_add(tgt, idx, src, axis=0):
    return tgt.clone().index_add_(axis, idx, src)
def _segment_sum(src, seg, n):
    out = torch.zeros((n,) + src.shape[1:], dtype=src.dtype)
    return out.index_add_(0, seg, src)
def _pad(x, pw):
    flat = tuple(v for pair in reversed(pw) for v in pair)
    return torch.nn.functional.pad(x, flat)

kernel_name = 'gated_mix__linear_kernel_hedgehog__dense_activation_silu__prompt'

def setup_inputs():

    _w = (lambda a: (lambda t: t.to(torch.bfloat16) if t.is_floating_point() else t)(torch.as_tensor(a)))
    def _A_setup():
        B, S, D, H = 1, 2048, 640, 32
        Dh = D // H
        return {
            "x": _w((lambda *s: torch.randn(*s, dtype=torch.bfloat16) * 0.1)(B, S, D)),
            "wqkv": _w((lambda *s: torch.randn(*s, dtype=torch.bfloat16) * 0.8)(D, 3 * D)),
            "w_phi": _w((lambda *s: torch.randn(*s, dtype=torch.bfloat16) * 0.8)(H, Dh, Dh) * (Dh ** -0.5)),
            "wo": _w((lambda *s: torch.randn(*s, dtype=torch.bfloat16) * 0.2)(D, D)),
        }
    _A_in = _A_setup()

    def _B_setup():
        B, S, D, F = 1, 2048, 640, 1728
        return {
            "x": _w((lambda *s: torch.randn(*s, dtype=torch.bfloat16) * 0.1)(B, S, D)),
            "w1": _w((lambda *s: torch.randn(*s, dtype=torch.bfloat16) * 3.2)(D, F) * (D ** -0.5)),
            "w2": _w((lambda *s: torch.randn(*s, dtype=torch.bfloat16) * 3.2)(F, D) * (F ** -0.5)),
        }
    _B_in = _B_setup()

    return {
        "A_x": _A_in["x"],
        "A_wqkv": _A_in["wqkv"],
        "A_w_phi": _A_in["w_phi"],
        "A_wo": _A_in["wo"],
        "B_x": _B_in["x"],
        "B_w1": _B_in["w1"],
        "B_w2": _B_in["w2"],
        "gate_w": _w((lambda *s: torch.randn(*s, dtype=torch.bfloat16) * 0.1)(640) * 16.0),
    }

def reference(A_x, A_wqkv, A_w_phi, A_wo, B_x, B_w1, B_w2, gate_w):
    def _A_ref(x, wqkv, w_phi, wo):
        B, S, D = x.shape
        H = 32; Dh = D // H
        qkv = (lambda x, *d: x.permute(*d))((x @ wqkv).reshape(B, S, 3, H, Dh), 2, 0, 3, 1, 4)
        q, k, v = qkv[0], qkv[1], qkv[2]
        pq = torch.einsum("bhsd,hdf->bhsf", q, w_phi)
        pk = torch.einsum("bhsd,hdf->bhsf", k, w_phi)
        phi_q = torch.concatenate([_softmax(pq, axis=-1), _softmax(-pq, axis=-1)], axis=-1)
        phi_k = torch.concatenate([_softmax(pk, axis=-1), _softmax(-pk, axis=-1)], axis=-1)
        kv = torch.einsum("bhsf,bhsd->bhfd", phi_k, v)
        z  = torch.einsum("bhsf->bhf", phi_k)
        num = torch.einsum("bhqf,bhfd->bhqd", phi_q, kv)
        den = torch.einsum("bhqf,bhf->bhq", phi_q, z)[..., None] + 1e-6
        o = num / den
        return (lambda x, *d: x.permute(*d))(o, 0, 2, 1, 3).reshape(B, S, D) @ wo

    def _B_ref(x, w1, w2):
        h = torch.einsum("bsd,df->bsf", x, w1)
        h = _silu(h)
        out = torch.einsum("bsf,fd->bsd", h, w2)
        return out

    x = A_x
    g = _sigmoid((x * gate_w).sum(-1, keepdims=True))
    a = _A_ref(x, A_wqkv, A_w_phi, A_wo)
    b = _B_ref(x, B_w1, B_w2)
    return g * a + (1 - g) * b
```

```cpp
#include <hip/hip_runtime.h>
#include <hip/hip_bf16.h>
#include <stdint.h>

typedef unsigned short u16;
static __device__ __forceinline__ float bf2f(u16 v) { return __uint_as_float(((unsigned)v) << 16); }
static __device__ __forceinline__ u16 f2bf(float f) { __hip_bfloat16 b = __float2bfloat16(f); return *(u16*)&b; }
static __device__ __forceinline__ float rbf(float f) { return bf2f(f2bf(f)); }

constexpr int S = 2048, D = 640, H = 32, DH = 20, F = 1728;

__global__ void naive_gemm(const u16* A, const u16* B, u16* C, int M, int N, int K) {
  int n = blockIdx.x * 64 + (threadIdx.x & 63);
  int m = blockIdx.y * 4 + (threadIdx.x >> 6);
  if (n >= N || m >= M) return;
  float acc = 0.f;
  for (int k = 0; k < K; ++k) acc += bf2f(A[m * K + k]) * bf2f(B[k * N + n]);
  C[m * N + n] = f2bf(acc);
}
__global__ void naive_phi(const u16* qkv, const u16* wphi, u16* phi) {
  int idx = blockIdx.x * 256 + threadIdx.x;
  if (idx >= S * 64) return;
  int s = idx / 64, w = (idx / 32) & 1, h = idx & 31;
  float p[DH];
  for (int f = 0; f < DH; ++f) {
    float acc = 0.f;
    for (int d = 0; d < DH; ++d) acc += bf2f(qkv[s * 1920 + w * 640 + h * 20 + d]) * bf2f(wphi[h * 400 + d * 20 + f]);
    p[f] = rbf(acc);
  }
  float mx = -1e30f, mn = 1e30f;
  for (int f = 0; f < DH; ++f) { mx = fmaxf(mx, p[f]); mn = fminf(mn, p[f]); }
  float s1 = 0.f, s2 = 0.f;
  for (int f = 0; f < DH; ++f) { s1 += expf(p[f] - mx); s2 += expf(mn - p[f]); }
  for (int f = 0; f < DH; ++f) {
    phi[(size_t)idx * 40 + f] = f2bf(expf(p[f] - mx) / s1);
    phi[(size_t)idx * 40 + 20 + f] = f2bf(expf(mn - p[f]) / s2);
  }
}
__global__ void naive_kv(const u16* qkv, const u16* phi, float* kv) {
  int idx = blockIdx.x * 64 + threadIdx.x;
  if (idx >= H * 40 * 21) return;
  int h = idx / 840, f = (idx / 21) % 40, d = idx % 21;
  float acc = 0.f;
  for (int s = 0; s < S; ++s) {
    float pk = bf2f(phi[((size_t)s * 64 + 32 + h) * 40 + f]);
    float v = d < 20 ? bf2f(qkv[s * 1920 + 1280 + h * 20 + d]) : 1.0f;
    acc += pk * v;
  }
  kv[idx] = rbf(acc);
}
__global__ void naive_o(const u16* phi, const float* kv, u16* o) {
  int idx = blockIdx.x * 256 + threadIdx.x;
  if (idx >= S * D) return;
  int s = idx / 640, h = (idx % 640) / 20, d = idx % 20;
  float num = 0.f, den = 0.f;
  for (int f = 0; f < 40; ++f) {
    float pq = bf2f(phi[((size_t)s * 64 + h) * 40 + f]);
    num += pq * kv[h * 840 + f * 21 + d];
    den += pq * kv[h * 840 + f * 21 + 20];
  }
  num = rbf(num); den = rbf(rbf(den) + 1e-6f);
  o[idx] = f2bf(num / den);
}
__global__ void naive_silu(u16* h) {
  int idx = blockIdx.x * 256 + threadIdx.x;
  if (idx >= S * F) return;
  float v = bf2f(h[idx]);
  h[idx] = f2bf(v / (1.f + expf(-v)));
}
__global__ void naive_mix(const u16* x, const u16* gw, const u16* a, const u16* b, u16* out) {
  int s = blockIdx.x;
  __shared__ float red[64];
  float acc = 0.f;
  for (int k = threadIdx.x; k < D; k += 64) acc += rbf(bf2f(x[s * D + k]) * bf2f(gw[k]));
  red[threadIdx.x] = acc;
  __syncthreads();
  float t = 0.f;
  for (int i = 0; i < 64; ++i) t += red[i];
  t = rbf(t);
  float g = rbf(1.f / (1.f + expf(-t)));
  float og = rbf(1.f - g);
  for (int k = threadIdx.x; k < D; k += 64) {
    float r = rbf(g * bf2f(a[s * D + k])) + rbf(og * bf2f(b[s * D + k]));
    out[s * D + k] = f2bf(r);
  }
}

extern "C" void kernel_launch(void* const* d_in, const int* in_sizes, int n_in,
                              void* d_out, int out_size, void* d_ws, size_t ws_size,
                              hipStream_t stream) {
  const u16* x = (const u16*)d_in[0];
  const u16* wqkv = (const u16*)d_in[1];
  const u16* wphi = (const u16*)d_in[2];
  const u16* wo = (const u16*)d_in[3];
  const u16* w1 = (const u16*)d_in[5];
  const u16* w2 = (const u16*)d_in[6];
  const u16* gw = (const u16*)d_in[7];
  char* ws = (char*)d_ws;
  u16* qkv = (u16*)ws; ws += (size_t)S * 1920 * 2;
  u16* phi = (u16*)ws; ws += (size_t)S * 64 * 40 * 2;
  float* kv = (float*)ws; ws += (size_t)H * 840 * 4;
  u16* o = (u16*)ws; ws += (size_t)S * D * 2;
  u16* h = (u16*)ws; ws += (size_t)S * F * 2;
  u16* a = (u16*)ws; ws += (size_t)S * D * 2;
  u16* b = (u16*)ws; ws += (size_t)S * D * 2;
  naive_gemm<<<dim3(1920 / 64, S / 4), 256, 0, stream>>>(x, wqkv, qkv, S, 1920, D);
  naive_phi<<<S * 64 / 256, 256, 0, stream>>>(qkv, wphi, phi);
  naive_kv<<<(H * 840 + 63) / 64, 64, 0, stream>>>(qkv, phi, kv);
  naive_o<<<S * D / 256, 256, 0, stream>>>(phi, kv, o);
  naive_gemm<<<dim3(D / 64, S / 4), 256, 0, stream>>>(o, wo, a, S, D, D);
  naive_gemm<<<dim3(F / 64, S / 4), 256, 0, stream>>>(x, w1, h, S, F, D);
  naive_silu<<<S * F / 256, 256, 0, stream>>>(h);
  naive_gemm<<<dim3(D / 64, S / 4), 256, 0, stream>>>(h, w2, b, S, D, F);
  naive_mix<<<S, 64, 0, stream>>>(x, gw, a, b, (u16*)d_out);
}
```

```cpp
#include <hip/hip_runtime.h>
#include <hip/hip_bf16.h>

__global__ __launch_bounds__(512, 2) void hedgehog_fused(const void* x, const void* wqkv, const void* wphi, const void* wo,
                                                          const void* w1, const void* w2, const void* gate, void* out, void* ws) {
  __shared__ __attribute__((aligned(16))) char smem[163840];
  const unsigned wave = __builtin_amdgcn_readfirstlane(threadIdx.x >> 6);
  asm volatile(
      "s_mov_b32 s64, %2\n"
      "s_mov_b32 s73, %1\n"
      "s_load_dwordx16 s[8:23], %0, 0x0\n"
      "s_load_dwordx2 s[24:25], %0, 0x40\n"
      "v_mbcnt_lo_u32_b32 v0, -1, 0\n"
      "v_mbcnt_hi_u32_b32 v0, -1, v0\n"
      "v_lshlrev_b32 v5, 4, v0\n"
      "s_and_b32 s74, s73, 7\n"
      "s_lshr_b32 s75, s73, 3\n"
      "s_and_b32 s65, s74, 1\n"
      "s_lshl_b32 s65, s65, 2\n"
      "s_and_b32 s76, s75, 3\n"
      "s_add_u32 s65, s65, s76\n"
      "s_lshr_b32 s70, s74, 1\n"
      "s_lshr_b32 s71, s75, 2\n"
      "s_lshl_b32 s66, s70, 3\n"
      "s_add_u32 s66, s66, s71\n"
      "v_bfe_u32 v26, v0, 4, 1\n"
      "v_lshrrev_b32 v27, 5, v0\n"
      "v_bfe_u32 v24, v0, 2, 2\n"
      "v_and_b32 v25, 3, v0\n"
      "s_sub_u32 s67, s66, s70\n"
      "s_cmp_eq_u32 s71, 7\n"
      "s_cselect_b32 s68, 1, 0\n"
      "s_cmp_eq_u32 s66, 30\n"
      "s_cselect_b32 s68, 1, s68\n"
      "s_mul_i32 s74, s70, 5\n"
      "s_lshr_b32 s74, s74, 2\n"
      "s_add_u32 s75, s65, s66\n"
      "s_and_b32 s75, s75, 1\n"
      "s_mul_i32 s75, s75, 5\n"
      "s_add_u32 s74, s74, s75\n"
      "s_lshr_b32 s75, s65, 2\n"
      "s_lshl_b32 s75, s75, 1\n"
      "s_add_u32 s74, s74, s75\n"
      "s_sub_u32 s75, s74, 10\n"
      "s_cmp_ge_u32 s74, 10\n"
      "s_cselect_b32 s69, s75, s74\n"
      "s_lshl_b32 s72, s69, 7\n"
      "s_mul_i32 s52, s69, 221184\n"
      "s_mul_i32 s27, s64, 12288\n"
      "s_add_u32 s27, s27, 65536\n"
      "s_lshl_b32 s53, s64, 10\n"
      "v_lshrrev_b32 v17, 3, v0\n"
      "v_and_b32 v18, 7, v0\n"
      "v_lshrrev_b32 v19, 4, v0\n"
      "s_lshl_b32 s74, s64, 5\n"
      "v_add_u32 v28, s74, v17\n"
      "v_mul_u32_u24 v28, 1280, v28\n"
      "v_xor_b32 v19, v19, v18\n"
      "v_lshl_add_u32 v8, v19, 4, v28\n"
      "v_xor_b32 v9, 64, v8\n"
      "v_add_u32 v9, 9216, v9\n"
      "v_add_u32 v10, 18432, v8\n"
      "v_add_u32 v11, 18432, v9\n"
      "s_lshl_b32 s74, s64, 3\n"
      "v_add_u32 v28, s74, v17\n"
      "v_mul_u32_u24 v28, 3456, v28\n"
      "v_lshlrev_b32 v19, 2, v26\n"
      "v_xor_b32 v19, v18, v19\n"
      "v_lshl_add_u32 v28, v19, 4, v28\n"
      "s_lshl_b32 s74, s67, 7\n"
      "v_add_u32 v1, s74, v28\n"
      "s_mov_b32 s30, 327680\n"
      "s_mov_b32 s31, 0x20000\n"
      "s_mov_b32 s47, 0x20000\n"
      "s_mov_b32 s46, 2211840\n"
      "s_mul_i32 s77, s65, 327680\n"
      "s_waitcnt lgkmcnt(0)\n"
      "s_load_dword s26, s[24:25], 0x0\n"
      "s_mov_b32 s44, s16\n"
      "s_and_b32 s45, s17, 0xffff\n"
      "s_add_u32 s28, s8, s77\n"
      "s_addc_u32 s29, s9, 0\n"
      "s_and_b32 s29, s29, 0xffff\n"
      "s_cmp_lg_u32 s68, 0\n"
      "s_cbranch_scc1 L_pre\n"
      "s_add_u32 m0, s53, 0\n"
      "s_setprio 0\n"
      "buffer_load_dwordx4 v1, s[44:47], s52 offen lds\n"
      "s_setprio 1\n"
      "s_add_u32 s52, s52, 221184\n"
      "s_cmp_eq_u32 s52, 2211840\n"
      "s_cselect_b32 s52, 0, s52\n"
      "s_add_u32 m0, s27, 0\n"
      "s_setprio 0\n"
      "buffer_load_dwordx4 v8, s[28:31], s72 offen lds\n"
      "s_setprio 1\n"
      "s_setprio 0\n"
      "buffer_load_dwordx4 v9, s[28:31], s72 offen offset:1024 lds\n"
      "s_setprio 1\n"
      "s_setprio 0\n"
      "buffer_load_dwordx4 v10, s[28:31], s72 offen offset:2048 lds\n"
      "s_setprio 1\n"
      "s_add_u32 m0, s27, 0\n"
      "s_setprio 0\n"
      "buffer_load_dwordx4 v11, s[28:31], s72 offen offset:3072 lds\n"
      "s_setprio 1\n"
      "s_add_u32 s72, s72, 128\n"
      "s_cmp_eq_u32 s72, 1280\n"
      "s_cselect_b32 s72, 0, s72\n"
      "s_cmp_lt_u32 s64, 7\n"
      "s_cbranch_scc1 L_noph0\n"
      "s_mul_i32 s73, s65, 27\n"
      "s_add_u32 s73, s73, s67\n"
      "s_mov_b64 s[40:41], s[10:11]\n"
      "s_mov_b32 s42, 2457600\n"
      "s_mul_i32 s74, s73, 11392\n"
      "s_mov_b32 s75, 33554431\n"
      "s_mov_b32 s76, 128\n"
      "s_mul_i32 s97, s73, 10240\n"
      "s_cmp_eq_u32 s64, 5\n"
      "s_cselect_b64 s[40:41], s[18:19], s[40:41]\n"
      "s_cselect_b32 s42, 2211840, s42\n"
      "s_cselect_b32 s74, s97, s74\n"
      "s_cselect_b32 s75, 65535, s75\n"
      "s_mul_i32 s97, s73, 3840\n"
      "s_cmp_eq_u32 s64, 6\n"
      "s_cselect_b64 s[40:41], s[14:15], s[40:41]\n"
      "s_cselect_b32 s42, 819200, s42\n"
      "s_cselect_b32 s74, s97, s74\n"
      "s_cselect_b32 s75, 0, s75\n"
      "s_getpc_b64 s[36:37]\n"
      "L_pc:\n"
      "s_mov_b32 s97, 0\n"
      "s_cmp_eq_u32 s64, 7\n"
      "s_cselect_b64 s[40:41], s[36:37], s[40:41]\n"
      "s_cselect_b32 s42, L_end-L_pc, s42\n"
      "s_cselect_b32 s74, s97, s74\n"
      "s_cselect_b32 s75, -1, s75\n"
      "s_and_b32 s41, s41, 0xffff\n"
      "s_mov_b32 s43, 0x20000\n"
      "v_mul_u32_u24 v254, s76, v0\n"
      "v_add_u32 v254, s74, v254\n"
      "s_cmp_eq_u32 s64, 6\n"
      "s_cselect_b64 exec, 0x3fffffff, -1\n"
      "buffer_load_dword v252, v254, s[40:43], 0 offen\n"
      "s_mov_b64 exec, -1\n"
      "s_cbranch_scc1 L_noph0\n"
      "s_lshl_b32 s76, s76, 6\n"
      "v_add_u32 v254, s76, v254\n"
      "s_mov_b32 exec_lo, s75\n"
      "s_cmp_eq_u32 s64, 7\n"
      "s_cselect_b32 exec_hi, -1, 0\n"
      "buffer_load_dword v253, v254, s[40:43], 0 offen\n"
      "s_mov_b64 exec, -1\n"
      "L_noph0:\n"
      "s_add_u32 m0, s53, 8192\n"
      "s_setprio 0\n"
      "buffer_load_dwordx4 v1, s[44:47], s52 offen lds\n"
      "s_setprio 1\n"
      "s_add_u32 s52, s52, 221184\n"
      "s_cmp_eq_u32 s52, 2211840\n"
      "s_cselect_b32 s52, 0, s52\n"
      "s_add_u32 m0, s27, 4096\n"
      "s_setprio 0\n"
      "buffer_load_dwordx4 v8, s[28:31], s72 offen lds\n"
      "s_setprio 1\n"
      "s_setprio 0\n"
      "buffer_load_dwordx4 v9, s[28:31], s72 offen offset:1024 lds\n"
      "s_setprio 1\n"
      "s_setprio 0\n"
      "buffer_load_dwordx4 v10, s[28:31], s72 offen offset:2048 lds\n"
      "s_setprio 1\n"
      "s_add_u32 m0, s27, 4096\n"
      "s_setprio 0\n"
      "buffer_load_dwordx4 v11, s[28:31], s72 offen offset:3072 lds\n"
      "s_setprio 1\n"
      "s_add_u32 s72, s72, 128\n"
      "s_cmp_eq_u32 s72, 1280\n"
      "s_cselect_b32 s72, 0, s72\n"
      "s_add_u32 m0, s53, 16384\n"
      "s_setprio 0\n"
      "buffer_load_dwordx4 v1, s[44:47], s52 offen lds\n"
      "s_setprio 1\n"
      "s_add_u32 s52, s52, 221184\n"
      "s_cmp_eq_u32 s52, 2211840\n"
      "s_cselect_b32 s52, 0, s52\n"
      "s_mov_b32 s40, s14\n"
      "s_and_b32 s41, s15, 0xffff\n"
      "s_mov_b32 s42, 819200\n"
      "s_mov_b32 s43, 0x20000\n"
      "s_mov_b32 s48, s18\n"
      "s_and_b32 s49, s19, 0xffff\n"
      "s_mov_b32 s50, 2211840\n"
      "s_mov_b32 s51, 0x20000\n"
      "s_mul_i32 s77, s65, 27\n"
      "s_add_u32 s77, s77, s67\n"
      "s_lshl_b32 s77, s77, 2\n"
      "s_lshr_b32 s76, s64, 1\n"
      "s_add_u32 s77, s77, s76\n"
      "s_mul_i32 s79, s77, 886\n"
      "s_lshr_b32 s79, s79, 16\n"
      "s_mul_i32 s73, s79, 74\n"
      "s_sub_u32 s78, s77, s73\n"
      "s_lshl_b32 s74, s78, 5\n"
      "s_mov_b32 s75, 20480\n"
      "s_mov_b32 s97, 5120\n"
      "s_mov_b32 s96, 10240\n"
      "s_sub_u32 s73, s78, 54\n"
      "s_mul_i32 s73, s73, 40\n"
      "s_cmp_ge_u32 s78, 54\n"
      "s_cselect_b32 s74, s73, s74\n"
      "s_cselect_b32 s75, 25600, s75\n"
      "s_cselect_b32 s97, 10240, s97\n"
      "s_cselect_b32 s96, 5120, s96\n"
      "s_cselect_b64 s[12:13], s[40:41], s[48:49]\n"
      "s_cselect_b64 s[14:15], s[42:43], s[50:51]\n"
      "s_sub_u32 s73, s78, 70\n"
      "s_mul_i32 s73, s73, 160\n"
      "s_add_u32 s73, s73, 16\n"
      "s_cmp_ge_u32 s78, 70\n"
      "s_cselect_b32 s74, s73, s74\n"
      "s_cselect_b32 s75, 102400, s75\n"
      "s_cselect_b32 s97, 51200, s97\n"
      "s_cselect_b32 s96, 25600, s96\n"
      "s_mul_i32 s74, s74, 1280\n"
      "s_lshl_b32 s73, s79, 7\n"
      "s_add_u32 s74, s74, s73\n"
      "s_and_b32 s76, s64, 1\n"
      "s_mul_i32 s75, s75, s76\n"
      "s_add_u32 s16, s74, s75\n"
      "s_add_u32 s17, s16, s97\n"
      "v_bfe_u32 v222, v0, 3, 2\n"
      "v_mul_u32_u24 v222, 1280, v222\n"
      "v_lshl_add_u32 v222, v18, 4, v222\n"
      "v_mad_u32_u24 v222, v27, s96, v222\n"
      "s_mul_i32 s75, s78, 5120\n"
      "s_add_u32 s75, s75, 4194304\n"
      "s_lshl_b32 s73, s79, 1\n"
      "s_add_u32 s73, s73, s76\n"
      "s_lshl_b32 s73, s73, 1\n"
      "s_add_u32 s73, s73, 0\n"
      "s_mul_i32 s74, s73, 52\n"
      "s_lshr_b32 s74, s74, 8\n"
      "s_mul_i32 s74, s74, 365\n"
      "s_add_u32 s73, s73, s74\n"
      "s_lshl_b32 s73, s73, 10\n"
      "s_add_u32 s18, s73, s75\n"
      "s_lshl_b32 s73, s79, 1\n"
      "s_add_u32 s73, s73, s76\n"
      "s_lshl_b32 s73, s73, 1\n"
      "s_add_u32 s73, s73, 1\n"
      "s_mul_i32 s74, s73, 52\n"
      "s_lshr_b32 s74, s74, 8\n"
      "s_mul_i32 s74, s74, 365\n"
      "s_add_u32 s73, s73, s74\n"
      "s_lshl_b32 s73, s73, 10\n"
      "s_add_u32 s19, s73, s75\n"
      "s_lshr_b32 s73, s64, 1\n"
      "s_lshl_b32 s73, s73, 12\n"
      "s_lshl_b32 s74, s76, 6\n"
      "s_add_u32 s73, s73, s74\n"
      "v_lshrrev_b32 v223, 4, v0\n"
      "v_lshl_add_u32 v223, v223, 3, v24\n"
      "v_lshlrev_b32 v223, 7, v223\n"
      "v_lshl_add_u32 v223, v25, 3, v223\n"
      "v_add_u32 v223, s73, v223\n"
      "s_lshl_b32 s55, s77, 2\n"
      "s_add_u32 s55, s55, 8192\n"
      "s_cmp_lt_u32 s77, 740\n"
      "s_cselect_b32 s14, s14, 0\n"
      "s_cselect_b32 s11, 23068672, 0\n"
      "s_lshl_b32 s54, s64, 11\n"
      "s_add_u32 s54, s54, 49152\n"
      "s_add_u32 m0, s54, 0\n"
      "s_setprio 0\n"
      "buffer_load_dwordx4 v222, s[12:15], s16 offen lds\n"
      "s_setprio 1\n"
      "s_add_u32 m0, s54, 1024\n"
      "s_setprio 0\n"
      "buffer_load_dwordx4 v222, s[12:15], s17 offen lds\n"
      "s_setprio 1\n"
      "s_mov_b32 s60, s24\n"
      "s_and_b32 s61, s25, 0xffff\n"
      "s_mov_b32 s62, 23068672\n"
      "s_mov_b32 s63, 0x20000\n"
      "s_mov_b32 s56, s22\n"
      "s_and_b32 s57, s23, 0xffff\n"
      "s_mov_b32 s58, 2621440\n"
      "s_mov_b32 s59, 0x20000\n"
      "s_lshl_b32 s34, s69, 13\n"
      "s_mul_i32 s35, s66, 81920\n"
      "s_add_u32 s35, s35, 1048576\n"
      "s_add_u32 s35, s35, s53\n"
      "v_and_b32 v22, 31, v0\n"
      "v_lshlrev_b32 v22, 7, v22\n"
      "v_add_u32 v22, s27, v22\n"
      "v_bfe_u32 v20, v0, 1, 3\n"
      "v_lshlrev_b32 v21, 2, v27\n"
      "v_xor_b32 v20, v20, v21\n"
      "v_lshl_add_u32 v12, v20, 4, v22\n"
      "v_xor_b32 v13, 16, v12\n"
      "v_xor_b32 v14, 32, v12\n"
      "v_xor_b32 v15, 48, v12\n"
      "v_lshl_add_u32 v22, v27, 5, v24\n"
      "v_lshlrev_b32 v22, 7, v22\n"
      "v_lshl_add_u32 v22, v26, 5, v22\n"
      "v_lshl_add_u32 v22, v25, 3, v22\n"
      "v_lshrrev_b32 v23, 1, v24\n"
      "v_lshl_add_u32 v3, v23, 6, v22\n"
      "v_xor_b32 v4, 64, v3\n"
      "s_waitcnt lgkmcnt(0)\n"
      "s_add_u32 s26, s26, 1\n"
      "v_mov_b32 v2, s26\n"
      "s_waitcnt vmcnt(8)\n"
      "s_barrier\n"
      "ds_read_b128 v[96:99], v12\n"
      "ds_read_b128 v[100:103], v13\n"
      "ds_read_b128 v[104:107], v14\n"
      "ds_read_b128 v[108:111], v15\n"
      "ds_read_b64_tr_b16 v[64:65], v3 offset:0\n"
      "ds_read_b64_tr_b16 v[66:67], v3 offset:512\n"
      "ds_read_b64_tr_b16 v[68:69], v4 offset:0\n"
      "ds_read_b64_tr_b16 v[70:71], v4 offset:512\n"
      "ds_read_b64_tr_b16 v[72:73], v3 offset:1024\n"
      "ds_read_b64_tr_b16 v[74:75], v3 offset:1536\n"
      "ds_read_b64_tr_b16 v[76:77], v4 offset:1024\n"
      "ds_read_b64_tr_b16 v[78:79], v4 offset:1536\n"
      "ds_read_b64_tr_b16 v[80:81], v3 offset:2048\n"
      "ds_read_b64_tr_b16 v[82:83], v3 offset:2560\n"
      "ds_read_b64_tr_b16 v[84:85], v4 offset:2048\n"
      "ds_read_b64_tr_b16 v[86:87], v4 offset:2560\n"
      "ds_read_b64_tr_b16 v[88:89], v3 offset:3072\n"
      "ds_read_b64_tr_b16 v[90:91], v3 offset:3584\n"
      "ds_read_b64_tr_b16 v[92:93], v4 offset:3072\n"
      "ds_read_b64_tr_b16 v[94:95], v4 offset:3584\n"
      "s_waitcnt lgkmcnt(15)\n"
      "s_waitcnt lgkmcnt(14)\n"
      "v_mfma_f32_32x32x16_bf16 v[32:47], v[64:67], v[96:99], 0\n"
      "s_add_u32 m0, s27, 8192\n"
      "s_setprio 0\n"
      "buffer_load_dwordx4 v8, s[28:31], s72 offen lds\n"
      "s_setprio 1\n"
      "s_waitcnt lgkmcnt(12)\n"
      "v_mfma_f32_32x32x16_bf16 v[48:63], v[68:71], v[96:99], 0\n"
      "s_add_u32 m0, s27, 8192\n"
      "s_setprio 0\n"
      "buffer_load_dwordx4 v9, s[28:31], s72 offen offset:1024 lds\n"
      "s_setprio 1\n"
      "s_waitcnt lgkmcnt(10)\n"
      "v_mfma_f32_32x32x16_bf16 v[32:47], v[72:75], v[100:103], v[32:47]\n"
      "s_add_u32 m0, s27, 8192\n"
      "s_setprio 0\n"
      "buffer_load_dwordx4 v10, s[28:31], s72 offen offset:2048 lds\n"
      "s_setprio 1\n"
      "s_waitcnt lgkmcnt(8)\n"
      "v_mfma_f32_32x32x16_bf16 v[48:63], v[76:79], v[100:103], v[48:63]\n"
      "s_add_u32 m0, s27, 8192\n"
      "s_setprio 0\n"
      "buffer_load_dwordx4 v11, s[28:31], s72 offen offset:3072 lds\n"
      "s_setprio 1\n"
      "s_add_u32 s72, s72, 128\n"
      "s_cmp_eq_u32 s72, 1280\n"
      "s_cselect_b32 s72, 0, s72\n"
      "s_waitcnt vmcnt(7)\n"
      "ds_read_b128 v[112:115], v12 offset:4096\n"
      "ds_read_b128 v[116:119], v13 offset:4096\n"
      "ds_read_b128 v[120:123], v14 offset:4096\n"
      "ds_read_b128 v[124:127], v15 offset:4096\n"
      "s_barrier\n"
      "ds_read_b64_tr_b16 v[64:65], v3 offset:8192\n"
      "ds_read_b64_tr_b16 v[66:67], v3 offset:8704\n"
      "ds_read_b64_tr_b16 v[68:69], v4 offset:8192\n"
      "ds_read_b64_tr_b16 v[70:71], v4 offset:8704\n"
      "ds_read_b64_tr_b16 v[72:73], v3 offset:9216\n"
      "ds_read_b64_tr_b16 v[74:75], v3 offset:9728\n"
      "ds_read_b64_tr_b16 v[76:77], v4 offset:9216\n"
      "ds_read_b64_tr_b16 v[78:79], v4 offset:9728\n"
      "s_waitcnt lgkmcnt(15)\n"
      "v_mfma_f32_32x32x16_bf16 v[32:47], v[80:83], v[104:107], v[32:47]\n"
      "s_add_u32 m0, s53, 24576\n"
      "s_setprio 0\n"
      "buffer_load_dwordx4 v1, s[44:47], s52 offen lds\n"
      "s_setprio 1\n"
      "s_add_u32 s52, s52, 221184\n"
      "s_cmp_eq_u32 s52, 2211840\n"
      "s_cselect_b32 s52, 0, s52\n"
      "v_mfma_f32_32x32x16_bf16 v[48:63], v[84:87], v[104:107], v[48:63]\n"
      "ds_read_b64_tr_b16 v[80:81], v3 offset:10240\n"
      "ds_read_b64_tr_b16 v[82:83], v3 offset:10752\n"
      "ds_read_b64_tr_b16 v[84:85], v4 offset:10240\n"
      "ds_read_b64_tr_b16 v[86:87], v4 offset:10752\n"
      "s_add_u32 m0, s53, 32768\n"
      "s_setprio 0\n"
      "buffer_load_dwordx4 v1, s[44:47], s52 offen lds\n"
      "s_setprio 1\n"
      "s_add_u32 s52, s52, 221184\n"
      "s_cmp_eq_u32 s52, 2211840\n"
      "s_cselect_b32 s52, 0, s52\n"
      "s_waitcnt lgkmcnt(12)\n"
      "v_mfma_f32_32x32x16_bf16 v[32:47], v[88:91], v[108:111], v[32:47]\n"
      "s_add_u32 m0, s27, 0\n"
      "s_setprio 0\n"
      "buffer_load_dwordx4 v8, s[28:31], s72 offen lds\n"
      "s_setprio 1\n"
      "v_mfma_f32_32x32x16_bf16 v[48:63], v[92:95], v[108:111], v[48:63]\n"
      "ds_read_b64_tr_b16 v[88:89], v3 offset:11264\n"
      "ds_read_b64_tr_b16 v[90:91], v3 offset:11776\n"
      "ds_read_b64_tr_b16 v[92:93], v4 offset:11264\n"
      "ds_read_b64_tr_b16 v[94:95], v4 offset:11776\n"
      "s_add_u32 m0, s27, 0\n"
      "s_setprio 0\n"
      "buffer_load_dwordx4 v9, s[28:31], s72 offen offset:1024 lds\n"
      "s_setprio 1\n"
      "s_waitcnt lgkmcnt(14)\n"
      "v_mfma_f32_32x32x16_bf16 v[32:47], v[64:67], v[112:115], v[32:47]\n"
      "s_add_u32 m0, s27, 0\n"
      "s_setprio 0\n"
      "buffer_load_dwordx4 v10, s[28:31], s72 offen offset:2048 lds\n"
      "s_setprio 1\n"
      "s_mov_b64 s[36:37], s[60:61]\n"
      "s_mov_b32 s39, 0x20000\n"
      "s_cmp_ge_u32 s64, 4\n"
      "s_cselect_b32 s38, 23068672, 0\n"
      "s_lshl_b32 s73, s65, 2\n"
      "s_lshr_b32 s74, s64, 1\n"
      "s_add_u32 s73, s73, s74\n"
      "s_and_b32 s76, s64, 1\n"
      "s_waitcnt lgkmcnt(12)\n"
      "v_mfma_f32_32x32x16_bf16 v[48:63], v[68:71], v[112:115], v[48:63]\n"
      "s_add_u32 m0, s27, 0\n"
      "s_setprio 0\n"
      "buffer_load_dwordx4 v11, s[28:31], s72 offen offset:3072 lds\n"
      "s_setprio 1\n"
      "s_add_u32 s72, s72, 128\n"
      "s_cmp_eq_u32 s72, 1280\n"
      "s_cselect_b32 s72, 0, s72\n"
      "s_mul_i32 s74, s73, 54\n"
      "s_lshl_b32 s75, s67, 1\n"
      "s_add_u32 s74, s74, s75\n"
      "s_lshl_b32 s74, s74, 12\n"
      "s_lshl_b32 s77, s76, 11\n"
      "s_add_u32 s74, s74, s77\n"
      "s_add_u32 s20, s74, 8388608\n"
      "s_waitcnt lgkmcnt(10)\n"
      "v_mfma_f32_32x32x16_bf16 v[32:47], v[72:75], v[116:119], v[32:47]\n"
      "s_lshl_b32 s21, s73, 8\n"
      "s_add_u32 s75, s75, s76\n"
      "s_lshl_b32 s75, s75, 2\n"
      "s_add_u32 s21, s21, s75\n"
      "s_add_u32 s21, s21, 12288\n"
      "s_mul_i32 s74, s73, 81920\n"
      "s_add_u32 s74, s74, s77\n"
      "s_waitcnt lgkmcnt(8)\n"
      "v_mfma_f32_32x32x16_bf16 v[48:63], v[76:79], v[116:119], v[48:63]\n"
      "s_and_b32 s75, s66, 1\n"
      "s_lshl_b32 s75, s75, 9\n"
      "s_add_u32 s74, s74, s75\n"
      "s_add_u32 s74, s74, 16777216\n"
      "s_lshr_b32 s75, s66, 1\n"
      "s_lshl_b32 s75, s75, 12\n"
      "s_add_u32 s24, s75, s74\n"
      "s_waitcnt vmcnt(6)\n"
      "ds_read_b128 v[128:131], v12 offset:8192\n"
      "ds_read_b128 v[132:135], v13 offset:8192\n"
      "ds_read_b128 v[136:139], v14 offset:8192\n"
      "ds_read_b128 v[140:143], v15 offset:8192\n"
      "s_barrier\n"
      "ds_read_b64_tr_b16 v[64:65], v3 offset:16384\n"
      "ds_read_b64_tr_b16 v[66:67], v3 offset:16896\n"
      "ds_read_b64_tr_b16 v[68:69], v4 offset:16384\n"
      "ds_read_b64_tr_b16 v[70:71], v4 offset:16896\n"
      "ds_read_b64_tr_b16 v[72:73], v3 offset:17408\n"
      "ds_read_b64_tr_b16 v[74:75], v3 offset:17920\n"
      "ds_read_b64_tr_b16 v[76:77], v4 offset:17408\n"
      "ds_read_b64_tr_b16 v[78:79], v4 offset:17920\n"
      "ds_read_b64_tr_b16 v[224:225], v223 offset:49152\n"
      "ds_read_b64_tr_b16 v[226:227], v223 offset:49664\n"
      "ds_read_b64_tr_b16 v[228:229], v223 offset:49184\n"
      "ds_read_b64_tr_b16 v[230:231], v223 offset:49696\n"
      "s_mov_b64 s[12:13], s[60:61]\n"
      "s_mov_b32 s14, s11\n"
      "s_waitcnt lgkmcnt(15)\n"
      "v_mfma_f32_32x32x16_bf16 v[32:47], v[80:83], v[120:123], v[32:47]\n"
      "s_add_u32 m0, s53, 40960\n"
      "s_setprio 0\n"
      "buffer_load_dwordx4 v1, s[44:47], s52 offen lds\n"
      "s_setprio 1\n"
      "s_add_u32 s52, s52, 221184\n"
      "s_cmp_eq_u32 s52, 2211840\n"
      "s_cselect_b32 s52, 0, s52\n"
      "s_lshl_b32 s77, s70, 12\n"
      "s_add_u32 s74, s74, s77\n"
      "s_lshr_b32 s77, s71, 1\n"
      "s_lshl_b32 s77, s77, 7\n"
      "s_add_u32 s74, s74, s77\n"
      "s_add_u32 s25, s74, 65536\n"
      "s_lshl_b32 s40, s73, 8\n"
      "v_mfma_f32_32x32x16_bf16 v[48:63], v[84:87], v[120:123], v[48:63]\n"
      "ds_read_b64_tr_b16 v[80:81], v3 offset:18432\n"
      "ds_read_b64_tr_b16 v[82:83], v3 offset:18944\n"
      "ds_read_b64_tr_b16 v[84:85], v4 offset:18432\n"
      "ds_read_b64_tr_b16 v[86:87], v4 offset:18944\n"
      "s_lshl_b32 s77, s66, 1\n"
      "s_add_u32 s77, s77, s76\n"
      "s_lshl_b32 s77, s77, 2\n"
      "s_add_u32 s40, s40, s77\n"
      "s_add_u32 s40, s40, 20480\n"
      "s_add_u32 s73, s65, s71\n"
      "s_and_b32 s73, s73, 0\n"
      "s_waitcnt lgkmcnt(15)\n"
      "s_waitcnt lgkmcnt(4)\n"
      "v_mfma_f32_32x32x16_bf16 v[32:47], v[88:91], v[124:127], v[32:47]\n"
      "s_add_u32 m0, s27, 4096\n"
      "s_setprio 0\n"
      "buffer_load_dwordx4 v8, s[28:31], s72 offen lds\n"
      "s_setprio 1\n"
      "s_lshl_b32 s73, s73, 2\n"
      "s_xor_b32 s54, s64, s73\n"
      "s_lshl_b32 s23, s66, 16\n"
      "s_add_u32 s23, s23, 20971520\n"
      "s_lshl_b32 s73, s65, 13\n"
      "s_add_u32 s22, s23, s73\n"
      "s_mul_i32 s73, s71, 74\n"
      "v_mfma_f32_32x32x16_bf16 v[48:63], v[92:95], v[124:127], v[48:63]\n"
      "ds_read_b64_tr_b16 v[88:89], v3 offset:19456\n"
      "ds_read_b64_tr_b16 v[90:91], v3 offset:19968\n"
      "ds_read_b64_tr_b16 v[92:93], v4 offset:19456\n"
      "ds_read_b64_tr_b16 v[94:95], v4 offset:19968\n"
      "s_add_u32 m0, s27, 4096\n"
      "s_setprio 0\n"
      "buffer_load_dwordx4 v9, s[28:31], s72 offen offset:1024 lds\n"
      "s_setprio 1\n"
      "s_add_u32 s74, s73, s54\n"
      "s_mul_i32 s75, s74, 5120\n"
      "s_add_u32 s79, s75, 4194304\n"
      "s_add_u32 s42, s75, 4532224\n"
      "s_add_u32 s74, s74, s54\n"
      "s_mul_i32 s74, s74, 5120\n"
      "s_add_u32 s41, s74, 4470784\n"
      "v_mfma_f32_32x32x16_bf16 v[32:47], v[64:67], v[128:131], v[32:47]\n"
      "s_add_u32 m0, s27, 4096\n"
      "s_setprio 0\n"
      "buffer_load_dwordx4 v10, s[28:31], s72 offen offset:2048 lds\n"
      "s_setprio 1\n"
      "s_lshl_b32 s73, s65, 2\n"
      "s_add_u32 s73, s73, s70\n"
      "s_mul_i32 s78, s73, 54\n"
      "s_add_u32 s78, s78, s54\n"
      "s_lshl_b32 s78, s78, 12\n"
      "s_add_u32 s78, s78, 8388608\n"
      "s_mul_i32 s77, s73, 81920\n"
      "v_mfma_f32_32x32x16_bf16 v[48:63], v[68:71], v[128:131], v[48:63]\n"
      "s_add_u32 m0, s27, 4096\n"
      "s_setprio 0\n"
      "buffer_load_dwordx4 v11, s[28:31], s72 offen offset:3072 lds\n"
      "s_setprio 1\n"
      "s_add_u32 s72, s72, 128\n"
      "s_cmp_eq_u32 s72, 1280\n"
      "s_cselect_b32 s72, 0, s72\n"
      "s_add_u32 s77, s77, 16777216\n"
      "s_lshl_b32 s74, s54, 13\n"
      "s_add_u32 s43, s74, s77\n"
      "s_lshl_b32 s74, s54, 12\n"
      "s_add_u32 s74, s74, s77\n"
      "s_add_u32 s48, s74, 49152\n"
      "s_mul_i32 s73, s73, 81920\n"
      "v_mfma_f32_32x32x16_bf16 v[32:47], v[72:75], v[132:135], v[32:47]\n"
      "s_setprio 0\n"
      "buffer_store_dwordx4 v[224:227], v5, s[12:15], s18 offen sc1\n"
      "s_setprio 1\n"
      "s_mul_i32 s74, s71, 160\n"
      "s_add_u32 s73, s73, s74\n"
      "s_add_u32 s74, s64, 0\n"
      "s_mul_i32 s75, s74, 52\n"
      "s_lshr_b32 s75, s75, 8\n"
      "s_mul_i32 s75, s75, 20320\n"
      "s_lshl_b32 s74, s74, 5\n"
      "v_mfma_f32_32x32x16_bf16 v[48:63], v[76:79], v[132:135], v[48:63]\n"
      "s_setprio 0\n"
      "buffer_store_dwordx4 v[228:231], v5, s[12:15], s19 offen sc1\n"
      "s_setprio 1\n"
      "s_add_u32 s74, s74, s75\n"
      "s_add_u32 s49, s74, s73\n"
      "s_add_u32 s74, s64, 8\n"
      "s_mul_i32 s75, s74, 52\n"
      "s_lshr_b32 s75, s75, 8\n"
      "s_mul_i32 s75, s75, 20320\n"
      "s_lshl_b32 s74, s74, 5\n"
      "s_waitcnt vmcnt(7)\n"
      "ds_read_b128 v[144:147], v12\n"
      "ds_read_b128 v[148:151], v13\n"
      "ds_read_b128 v[152:155], v14\n"
      "ds_read_b128 v[156:159], v15\n"
      "s_barrier\n"
      "ds_read_b64_tr_b16 v[64:65], v3 offset:24576\n"
      "ds_read_b64_tr_b16 v[66:67], v3 offset:25088\n"
      "ds_read_b64_tr_b16 v[68:69], v4 offset:24576\n"
      "ds_read_b64_tr_b16 v[70:71], v4 offset:25088\n"
      "ds_read_b64_tr_b16 v[72:73], v3 offset:25600\n"
      "ds_read_b64_tr_b16 v[74:75], v3 offset:26112\n"
      "ds_read_b64_tr_b16 v[76:77], v4 offset:25600\n"
      "ds_read_b64_tr_b16 v[78:79], v4 offset:26112\n"
      "s_waitcnt lgkmcnt(15)\n"
      "v_mfma_f32_32x32x16_bf16 v[32:47], v[80:83], v[136:139], v[32:47]\n"
      "s_add_u32 m0, s53, 49152\n"
      "s_setprio 0\n"
      "buffer_load_dwordx4 v1, s[44:47], s52 offen lds\n"
      "s_setprio 1\n"
      "s_add_u32 s52, s52, 221184\n"
      "s_cmp_eq_u32 s52, 2211840\n"
      "s_cselect_b32 s52, 0, s52\n"
      "s_add_u32 s74, s74, s75\n"
      "s_add_u32 s50, s74, s73\n"
      "s_add_u32 s74, s64, 16\n"
      "s_mul_i32 s75, s74, 52\n"
      "s_lshr_b32 s75, s75, 8\n"
      "s_mul_i32 s75, s75, 20320\n"
      "s_lshl_b32 s74, s74, 5\n"
      "v_mfma_f32_32x32x16_bf16 v[48:63], v[84:87], v[136:139], v[48:63]\n"
      "ds_read_b64_tr_b16 v[80:81], v3 offset:26624\n"
      "ds_read_b64_tr_b16 v[82:83], v3 offset:27136\n"
      "ds_read_b64_tr_b16 v[84:85], v4 offset:26624\n"
      "ds_read_b64_tr_b16 v[86:87], v4 offset:27136\n"
      "s_add_u32 s74, s74, s75\n"
      "s_add_u32 s51, s74, s73\n"
      "s_waitcnt lgkmcnt(12)\n"
      "v_mfma_f32_32x32x16_bf16 v[32:47], v[88:91], v[140:143], v[32:47]\n"
      "s_add_u32 m0, s27, 8192\n"
      "s_setprio 0\n"
      "buffer_load_dwordx4 v8, s[28:31], s72 offen lds\n"
      "s_setprio 1\n"
      "v_mfma_f32_32x32x16_bf16 v[48:63], v[92:95], v[140:143], v[48:63]\n"
      "ds_read_b64_tr_b16 v[88:89], v3 offset:27648\n"
      "ds_read_b64_tr_b16 v[90:91], v3 offset:28160\n"
      "ds_read_b64_tr_b16 v[92:93], v4 offset:27648\n"
      "ds_read_b64_tr_b16 v[94:95], v4 offset:28160\n"
      "s_add_u32 m0, s27, 8192\n"
      "s_setprio 0\n"
      "buffer_load_dwordx4 v9, s[28:31], s72 offen offset:1024 lds\n"
      "s_setprio 1\n"
      "s_waitcnt lgkmcnt(14)\n"
      "v_mfma_f32_32x32x16_bf16 v[32:47], v[64:67], v[144:147], v[32:47]\n"
      "s_add_u32 m0, s27, 8192\n"
      "s_setprio 0\n"
      "buffer_load_dwordx4 v10, s[28:31], s72 offen offset:2048 lds\n"
      "s_setprio 1\n"
      "s_waitcnt lgkmcnt(12)\n"
      "v_mfma_f32_32x32x16_bf16 v[48:63], v[68:71], v[144:147], v[48:63]\n"
      "s_add_u32 m0, s27, 8192\n"
      "s_setprio 0\n"
      "buffer_load_dwordx4 v11, s[28:31], s72 offen offset:3072 lds\n"
      "s_setprio 1\n"
      "s_add_u32 s72, s72, 128\n"
      "s_cmp_eq_u32 s72, 1280\n"
      "s_cselect_b32 s72, 0, s72\n"
      "s_waitcnt lgkmcnt(10)\n"
      "v_mfma_f32_32x32x16_bf16 v[32:47], v[72:75], v[148:151], v[32:47]\n"
      "s_waitcnt lgkmcnt(8)\n"
      "v_mfma_f32_32x32x16_bf16 v[48:63], v[76:79], v[148:151], v[48:63]\n"
      "s_waitcnt vmcnt(7)\n"
      "ds_read_b128 v[160:163], v12 offset:4096\n"
      "ds_read_b128 v[164:167], v13 offset:4096\n"
      "ds_read_b128 v[168:171], v14 offset:4096\n"
      "ds_read_b128 v[172:175], v15 offset:4096\n"
      "s_barrier\n"
      "ds_read_b64_tr_b16 v[64:65], v3 offset:32768\n"
      "ds_read_b64_tr_b16 v[66:67], v3 offset:33280\n"
      "ds_read_b64_tr_b16 v[68:69], v4 offset:32768\n"
      "ds_read_b64_tr_b16 v[70:71], v4 offset:33280\n"
      "ds_read_b64_tr_b16 v[72:73], v3 offset:33792\n"
      "ds_read_b64_tr_b16 v[74:75], v3 offset:34304\n"
      "ds_read_b64_tr_b16 v[76:77], v4 offset:33792\n"
      "ds_read_b64_tr_b16 v[78:79], v4 offset:34304\n"
      "s_waitcnt lgkmcnt(15)\n"
      "v_mfma_f32_32x32x16_bf16 v[32:47], v[80:83], v[152:155], v[32:47]\n"
      "s_add_u32 m0, s53, 57344\n"
      "s_setprio 0\n"
      "buffer_load_dwordx4 v1, s[44:47], s52 offen lds\n"
      "s_setprio 1\n"
      "s_add_u32 s52, s52, 221184\n"
      "s_cmp_eq_u32 s52, 2211840\n"
      "s_cselect_b32 s52, 0, s52\n"
      "v_mfma_f32_32x32x16_bf16 v[48:63], v[84:87], v[152:155], v[48:63]\n"
      "ds_read_b64_tr_b16 v[80:81], v3 offset:34816\n"
      "ds_read_b64_tr_b16 v[82:83], v3 offset:35328\n"
      "ds_read_b64_tr_b16 v[84:85], v4 offset:34816\n"
      "ds_read_b64_tr_b16 v[86:87], v4 offset:35328\n"
      "s_waitcnt lgkmcnt(12)\n"
      "v_mfma_f32_32x32x16_bf16 v[32:47], v[88:91], v[156:159], v[32:47]\n"
      "s_add_u32 m0, s27, 0\n"
      "s_setprio 0\n"
      "buffer_load_dwordx4 v8, s[28:31], s72 offen lds\n"
      "s_setprio 1\n"
      "v_mfma_f32_32x32x16_bf16 v[48:63], v[92:95], v[156:159], v[48:63]\n"
      "ds_read_b64_tr_b16 v[88:89], v3 offset:35840\n"
      "ds_read_b64_tr_b16 v[90:91], v3 offset:36352\n"
      "ds_read_b64_tr_b16 v[92:93], v4 offset:35840\n"
      "ds_read_b64_tr_b16 v[94:95], v4 offset:36352\n"
      "s_add_u32 m0, s27, 0\n"
      "s_setprio 0\n"
      "buffer_load_dwordx4 v9, s[28:31], s72 offen offset:1024 lds\n"
      "s_setprio 1\n"
      "s_waitcnt lgkmcnt(14)\n"
      "v_mfma_f32_32x32x16_bf16 v[32:47], v[64:67], v[160:163], v[32:47]\n"
      "s_add_u32 m0, s27, 0\n"
      "s_setprio 0\n"
      "buffer_load_dwordx4 v10, s[28:31], s72 offen offset:2048 lds\n"
      "s_setprio 1\n"
      "s_waitcnt lgkmcnt(12)\n"
      "v_mfma_f32_32x32x16_bf16 v[48:63], v[68:71], v[160:163], v[48:63]\n"
      "s_add_u32 m0, s27, 0\n"
      "s_setprio 0\n"
      "buffer_load_dwordx4 v11, s[28:31], s72 offen offset:3072 lds\n"
      "s_setprio 1\n"
      "s_add_u32 s72, s72, 128\n"
      "s_cmp_eq_u32 s72, 1280\n"
      "s_cselect_b32 s72, 0, s72\n"
      "s_waitcnt lgkmcnt(10)\n"
      "v_mfma_f32_32x32x16_bf16 v[32:47], v[72:75], v[164:167], v[32:47]\n"
      "s_waitcnt lgkmcnt(8)\n"
      "v_mfma_f32_32x32x16_bf16 v[48:63], v[76:79], v[164:167], v[48:63]\n"
      "s_waitcnt vmcnt(5)\n"
      "ds_read_b128 v[176:179], v12 offset:8192\n"
      "ds_read_b128 v[180:183], v13 offset:8192\n"
      "ds_read_b128 v[184:187], v14 offset:8192\n"
      "ds_read_b128 v[188:191], v15 offset:8192\n"
      "s_barrier\n"
      "ds_read_b64_tr_b16 v[64:65], v3 offset:40960\n"
      "ds_read_b64_tr_b16 v[66:67], v3 offset:41472\n"
      "ds_read_b64_tr_b16 v[68:69], v4 offset:40960\n"
      "ds_read_b64_tr_b16 v[70:71], v4 offset:41472\n"
      "ds_read_b64_tr_b16 v[72:73], v3 offset:41984\n"
      "ds_read_b64_tr_b16 v[74:75], v3 offset:42496\n"
      "ds_read_b64_tr_b16 v[76:77], v4 offset:41984\n"
      "ds_read_b64_tr_b16 v[78:79], v4 offset:42496\n"
      "s_and_b32 s97, s64, 1\n"
      "s_cselect_b32 s14, 0, s14\n"
      "s_mov_b64 exec, 1\n"
      "buffer_store_dword v2, off, s[12:15], s55 sc1\n"
      "s_mov_b64 exec, -1\n"
      "s_waitcnt lgkmcnt(15)\n"
      "v_mfma_f32_32x32x16_bf16 v[32:47], v[80:83], v[168:171], v[32:47]\n"
      "s_add_u32 m0, s53, 0\n"
      "s_setprio 0\n"
      "buffer_load_dwordx4 v1, s[44:47], s52 offen lds\n"
      "s_setprio 1\n"
      "s_add_u32 s52, s52, 221184\n"
      "s_cmp_eq_u32 s52, 2211840\n"
      "s_cselect_b32 s52, 0, s52\n"
      "v_mfma_f32_32x32x16_bf16 v[48:63], v[84:87], v[168:171], v[48:63]\n"
      "ds_read_b64_tr_b16 v[80:81], v3 offset:43008\n"
      "ds_read_b64_tr_b16 v[82:83], v3 offset:43520\n"
      "ds_read_b64_tr_b16 v[84:85], v4 offset:43008\n"
      "ds_read_b64_tr_b16 v[86:87], v4 offset:43520\n"
      "s_waitcnt lgkmcnt(12)\n"
      "v_mfma_f32_32x32x16_bf16 v[32:47], v[88:91], v[172:175], v[32:47]\n"
      "s_add_u32 m0, s27, 4096\n"
      "s_setprio 0\n"
      "buffer_load_dwordx4 v8, s[28:31], s72 offen lds\n"
      "s_setprio 1\n"
      "v_mfma_f32_32x32x16_bf16 v[48:63], v[92:95], v[172:175], v[48:63]\n"
      "ds_read_b64_tr_b16 v[88:89], v3 offset:44032\n"
      "ds_read_b64_tr_b16 v[90:91], v3 offset:44544\n"
      "ds_read_b64_tr_b16 v[92:93], v4 offset:44032\n"
      "ds_read_b64_tr_b16 v[94:95], v4 offset:44544\n"
      "s_add_u32 m0, s27, 4096\n"
      "s_setprio 0\n"
      "buffer_load_dwordx4 v9, s[28:31], s72 offen offset:1024 lds\n"
      "s_setprio 1\n"
      "s_waitcnt lgkmcnt(14)\n"
      "v_mfma_f32_32x32x16_bf16 v[32:47], v[64:67], v[176:179], v[32:47]\n"
      "s_add_u32 m0, s27, 4096\n"
      "s_setprio 0\n"
      "buffer_load_dwordx4 v10, s[28:31], s72 offen offset:2048 lds\n"
      "s_setprio 1\n"
      "s_waitcnt lgkmcnt(12)\n"
      "v_mfma_f32_32x32x16_bf16 v[48:63], v[68:71], v[176:179], v[48:63]\n"
      "s_add_u32 m0, s27, 4096\n"
      "s_setprio 0\n"
      "buffer_load_dwordx4 v11, s[28:31], s72 offen offset:3072 lds\n"
      "s_setprio 1\n"
      "s_add_u32 s72, s72, 128\n"
      "s_cmp_eq_u32 s72, 1280\n"
      "s_cselect_b32 s72, 0, s72\n"
      "s_waitcnt lgkmcnt(10)\n"
      "v_mfma_f32_32x32x16_bf16 v[32:47], v[72:75], v[180:183], v[32:47]\n"
      "s_waitcnt lgkmcnt(8)\n"
      "v_mfma_f32_32x32x16_bf16 v[48:63], v[76:79], v[180:183], v[48:63]\n"
      "s_waitcnt vmcnt(6)\n"
      "ds_read_b128 v[192:195], v12\n"
      "ds_read_b128 v[196:199], v13\n"
      "ds_read_b128 v[200:203], v14\n"
      "ds_read_b128 v[204:207], v15\n"
      "s_barrier\n"
      "ds_read_b64_tr_b16 v[64:65], v3 offset:49152\n"
      "ds_read_b64_tr_b16 v[66:67], v3 offset:49664\n"
      "ds_read_b64_tr_b16 v[68:69], v4 offset:49152\n"
      "ds_read_b64_tr_b16 v[70:71], v4 offset:49664\n"
      "ds_read_b64_tr_b16 v[72:73], v3 offset:50176\n"
      "ds_read_b64_tr_b16 v[74:75], v3 offset:50688\n"
      "ds_read_b64_tr_b16 v[76:77], v4 offset:50176\n"
      "ds_read_b64_tr_b16 v[78:79], v4 offset:50688\n"
      "s_waitcnt lgkmcnt(15)\n"
      "v_mfma_f32_32x32x16_bf16 v[32:47], v[80:83], v[184:187], v[32:47]\n"
      "s_add_u32 m0, s53, 8192\n"
      "s_setprio 0\n"
      "buffer_load_dwordx4 v1, s[44:47], s52 offen lds\n"
      "s_setprio 1\n"
      "s_add_u32 s52, s52, 221184\n"
      "s_cmp_eq_u32 s52, 2211840\n"
      "s_cselect_b32 s52, 0, s52\n"
      "v_mfma_f32_32x32x16_bf16 v[48:63], v[84:87], v[184:187], v[48:63]\n"
      "ds_read_b64_tr_b16 v[80:81], v3 offset:51200\n"
      "ds_read_b64_tr_b16 v[82:83], v3 offset:51712\n"
      "ds_read_b64_tr_b16 v[84:85], v4 offset:51200\n"
      "ds_read_b64_tr_b16 v[86:87], v4 offset:51712\n"
      "s_waitcnt lgkmcnt(12)\n"
      "v_mfma_f32_32x32x16_bf16 v[32:47], v[88:91], v[188:191], v[32:47]\n"
      "s_add_u32 m0, s27, 8192\n"
      "s_setprio 0\n"
      "buffer_load_dwordx4 v8, s[28:31], s72 offen lds\n"
      "s_setprio 1\n"
      "v_mfma_f32_32x32x16_bf16 v[48:63], v[92:95], v[188:191], v[48:63]\n"
      "ds_read_b64_tr_b16 v[88:89], v3 offset:52224\n"
      "ds_read_b64_tr_b16 v[90:91], v3 offset:52736\n"
      "ds_read_b64_tr_b16 v[92:93], v4 offset:52224\n"
      "ds_read_b64_tr_b16 v[94:95], v4 offset:52736\n"
      "s_add_u32 m0, s27, 8192\n"
      "s_setprio 0\n"
      "buffer_load_dwordx4 v9, s[28:31], s72 offen offset:1024 lds\n"
      "s_setprio 1\n"
      "s_waitcnt lgkmcnt(14)\n"
      "v_mfma_f32_32x32x16_bf16 v[32:47], v[64:67], v[192:195], v[32:47]\n"
      "s_add_u32 m0, s27, 8192\n"
      "s_setprio 0\n"
      "buffer_load_dwordx4 v10, s[28:31], s72 offen offset:2048 lds\n"
      "s_setprio 1\n"
      "s_waitcnt lgkmcnt(12)\n"
      "v_mfma_f32_32x32x16_bf16 v[48:63], v[68:71], v[192:195], v[48:63]\n"
      "s_add_u32 m0, s27, 8192\n"
      "s_setprio 0\n"
      "buffer_load_dwordx4 v11, s[28:31], s72 offen offset:3072 lds\n"
      "s_setprio 1\n"
      "s_add_u32 s72, s72, 128\n"
      "s_cmp_eq_u32 s72, 1280\n"
      "s_cselect_b32 s72, 0, s72\n"
      "s_waitcnt lgkmcnt(10)\n"
      "v_mfma_f32_32x32x16_bf16 v[32:47], v[72:75], v[196:199], v[32:47]\n"
      "s_waitcnt lgkmcnt(8)\n"
      "v_mfma_f32_32x32x16_bf16 v[48:63], v[76:79], v[196:199], v[48:63]\n"
      "s_waitcnt vmcnt(5)\n"
      "ds_read_b128 v[208:211], v12 offset:4096\n"
      "ds_read_b128 v[212:215], v13 offset:4096\n"
      "ds_read_b128 v[216:219], v14 offset:4096\n"
      "ds_read_b128 v[220:223], v15 offset:4096\n"
      "s_barrier\n"
      "ds_read_b64_tr_b16 v[64:65], v3 offset:57344\n"
      "ds_read_b64_tr_b16 v[66:67], v3 offset:57856\n"
      "ds_read_b64_tr_b16 v[68:69], v4 offset:57344\n"
      "ds_read_b64_tr_b16 v[70:71], v4 offset:57856\n"
      "ds_read_b64_tr_b16 v[72:73], v3 offset:58368\n"
      "ds_read_b64_tr_b16 v[74:75], v3 offset:58880\n"
      "ds_read_b64_tr_b16 v[76:77], v4 offset:58368\n"
      "ds_read_b64_tr_b16 v[78:79], v4 offset:58880\n"
      "s_cmp_lg_u32 s64, 3\n"
      "s_cbranch_scc1 Lai_1\n"
      "s_lshl_b32 s33, s66, 7\n"
      "s_add_u32 s33, s33, 4096\n"
      "v_min_u32 v16, 19, v0\n"
      "v_lshlrev_b32 v16, 2, v16\n"
      "buffer_load_dword v17, v16, s[60:63], s33 offen sc1\n"
      "Lai_1:\n"
      "s_waitcnt lgkmcnt(15)\n"
      "v_mfma_f32_32x32x16_bf16 v[32:47], v[80:83], v[200:203], v[32:47]\n"
      "v_mfma_f32_32x32x16_bf16 v[48:63], v[84:87], v[200:203], v[48:63]\n"
      "ds_read_b64_tr_b16 v[80:81], v3 offset:59392\n"
      "ds_read_b64_tr_b16 v[82:83], v3 offset:59904\n"
      "ds_read_b64_tr_b16 v[84:85], v4 offset:59392\n"
      "ds_read_b64_tr_b16 v[86:87], v4 offset:59904\n"
      "s_waitcnt lgkmcnt(12)\n"
      "v_mfma_f32_32x32x16_bf16 v[32:47], v[88:91], v[204:207], v[32:47]\n"
      "s_add_u32 m0, s27, 0\n"
      "s_setprio 0\n"
      "buffer_load_dwordx4 v8, s[28:31], s72 offen lds\n"
      "s_setprio 1\n"
      "v_mfma_f32_32x32x16_bf16 v[48:63], v[92:95], v[204:207], v[48:63]\n"
      "ds_read_b64_tr_b16 v[88:89], v3 offset:60416\n"
      "ds_read_b64_tr_b16 v[90:91], v3 offset:60928\n"
      "ds_read_b64_tr_b16 v[92:93], v4 offset:60416\n"
      "ds_read_b64_tr_b16 v[94:95], v4 offset:60928\n"
      "s_add_u32 m0, s27, 0\n"
      "s_setprio 0\n"
      "buffer_load_dwordx4 v9, s[28:31], s72 offen offset:1024 lds\n"
      "s_setprio 1\n"
      "s_waitcnt lgkmcnt(14)\n"
      "v_mfma_f32_32x32x16_bf16 v[32:47], v[64:67], v[208:211], v[32:47]\n"
      "s_add_u32 m0, s27, 0\n"
      "s_setprio 0\n"
      "buffer_load_dwordx4 v10, s[28:31], s72 offen offset:2048 lds\n"
      "s_setprio 1\n"
      "s_waitcnt lgkmcnt(12)\n"
      "v_mfma_f32_32x32x16_bf16 v[48:63], v[68:71], v[208:211], v[48:63]\n"
      "s_add_u32 m0, s27, 0\n"
      "s_setprio 0\n"
      "buffer_load_dwordx4 v11, s[28:31], s72 offen offset:3072 lds\n"
      "s_setprio 1\n"
      "s_add_u32 s72, s72, 128\n"
      "s_cmp_eq_u32 s72, 1280\n"
      "s_cselect_b32 s72, 0, s72\n"
      "s_waitcnt lgkmcnt(10)\n"
      "v_mfma_f32_32x32x16_bf16 v[32:47], v[72:75], v[212:215], v[32:47]\n"
      "s_waitcnt lgkmcnt(8)\n"
      "v_mfma_f32_32x32x16_bf16 v[48:63], v[76:79], v[212:215], v[48:63]\n"
      "s_waitcnt vmcnt(4)\n"
      "ds_read_b128 v[224:227], v12 offset:8192\n"
      "ds_read_b128 v[228:231], v13 offset:8192\n"
      "ds_read_b128 v[232:235], v14 offset:8192\n"
      "ds_read_b128 v[236:239], v15 offset:8192\n"
      "s_cmp_lg_u32 s64, 3\n"
      "s_cbranch_scc1 Lacd_4\n"
      "s_waitcnt vmcnt(4)\n"
      "s_mov_b32 s96, 20000\n"
      "Lac_3:\n"
      "v_cmp_ne_u32 vcc, s26, v17\n"
      "s_cmp_eq_u64 vcc, 0\n"
      "s_cbranch_scc1 Lacd_4\n"
      "s_sub_u32 s96, s96, 1\n"
      "s_cmp_eq_u32 s96, 0\n"
      "s_cbranch_scc1 Lacd_4\n"
      "s_sleep 4\n"
      "buffer_load_dword v17, v16, s[60:63], s33 offen sc1\n"
      "s_waitcnt vmcnt(0)\n"
      "s_branch Lac_3\n"
      "Lacd_4:\n"
      "s_barrier\n"
      "ds_read_b64_tr_b16 v[64:65], v3 offset:0\n"
      "ds_read_b64_tr_b16 v[66:67], v3 offset:512\n"
      "ds_read_b64_tr_b16 v[68:69], v4 offset:0\n"
      "ds_read_b64_tr_b16 v[70:71], v4 offset:512\n"
      "ds_read_b64_tr_b16 v[72:73], v3 offset:1024\n"
      "ds_read_b64_tr_b16 v[74:75], v3 offset:1536\n"
      "ds_read_b64_tr_b16 v[76:77], v4 offset:1024\n"
      "ds_read_b64_tr_b16 v[78:79], v4 offset:1536\n"
      "s_waitcnt lgkmcnt(15)\n"
      "v_mfma_f32_32x32x16_bf16 v[32:47], v[80:83], v[216:219], v[32:47]\n"
      "s_add_u32 m0, s53, 16384\n"
      "s_add_u32 s97, s34, s35\n"
      "s_setprio 0\n"
      "buffer_load_dwordx4 v5, s[60:63], s97 offen sc1 lds\n"
      "s_setprio 1\n"
      "s_add_u32 s34, s34, 8192\n"
      "s_cmp_eq_u32 s34, 81920\n"
      "s_cselect_b32 s34, 0, s34\n"
      "v_mfma_f32_32x32x16_bf16 v[48:63], v[84:87], v[216:219], v[48:63]\n"
      "ds_read_b64_tr_b16 v[80:81], v3 offset:2048\n"
      "ds_read_b64_tr_b16 v[82:83], v3 offset:2560\n"
      "ds_read_b64_tr_b16 v[84:85], v4 offset:2048\n"
      "ds_read_b64_tr_b16 v[86:87], v4 offset:2560\n"
      "s_add_u32 m0, s53, 24576\n"
      "s_add_u32 s97, s34, s35\n"
      "s_setprio 0\n"
      "buffer_load_dwordx4 v5, s[60:63], s97 offen sc1 lds\n"
      "s_setprio 1\n"
      "s_add_u32 s34, s34, 8192\n"
      "s_cmp_eq_u32 s34, 81920\n"
      "s_cselect_b32 s34, 0, s34\n"
      "s_waitcnt lgkmcnt(12)\n"
      "v_mfma_f32_32x32x16_bf16 v[32:47], v[88:91], v[220:223], v[32:47]\n"
      "v_mfma_f32_32x32x16_bf16 v[48:63], v[92:95], v[220:223], v[48:63]\n"
      "ds_read_b64_tr_b16 v[88:89], v3 offset:3072\n"
      "ds_read_b64_tr_b16 v[90:91], v3 offset:3584\n"
      "ds_read_b64_tr_b16 v[92:93], v4 offset:3072\n"
      "ds_read_b64_tr_b16 v[94:95], v4 offset:3584\n"
      "s_waitcnt lgkmcnt(14)\n"
      "v_mfma_f32_32x32x16_bf16 v[32:47], v[64:67], v[224:227], v[32:47]\n"
      "s_waitcnt lgkmcnt(12)\n"
      "v_mfma_f32_32x32x16_bf16 v[48:63], v[68:71], v[224:227], v[48:63]\n"
      "s_waitcnt lgkmcnt(10)\n"
      "v_mfma_f32_32x32x16_bf16 v[32:47], v[72:75], v[228:231], v[32:47]\n"
      "s_waitcnt lgkmcnt(8)\n"
      "v_mfma_f32_32x32x16_bf16 v[48:63], v[76:79], v[228:231], v[48:63]\n"
      "s_waitcnt vmcnt(2)\n"
      "ds_read_b128 v[240:243], v12\n"
      "ds_read_b128 v[244:247], v13\n"
      "ds_read_b128 v[248:251], v14\n"
      "ds_read_b128 v[252:255], v15\n"
      "s_barrier\n"
      "ds_read_b64_tr_b16 v[64:65], v3 offset:8192\n"
      "ds_read_b64_tr_b16 v[66:67], v3 offset:8704\n"
      "ds_read_b64_tr_b16 v[68:69], v4 offset:8192\n"
      "ds_read_b64_tr_b16 v[70:71], v4 offset:8704\n"
      "ds_read_b64_tr_b16 v[72:73], v3 offset:9216\n"
      "ds_read_b64_tr_b16 v[74:75], v3 offset:9728\n"
      "ds_read_b64_tr_b16 v[76:77], v4 offset:9216\n"
      "ds_read_b64_tr_b16 v[78:79], v4 offset:9728\n"
      "s_cmp_lg_u32 s64, 7\n"
      "s_cbranch_scc1 Lai_5\n"
      "s_mul_i32 s33, s71, 80\n"
      "s_lshr_b32 s33, s33, 6\n"
      "s_mul_i32 s33, s33, 296\n"
      "s_add_u32 s33, s33, 8192\n"
      "v_min_u32 v16, 36, v0\n"
      "v_lshlrev_b32 v16, 4, v16\n"
      "buffer_load_dwordx4 v[18:21], v16, s[60:63], s33 offen sc1\n"
      "Lai_5:\n"
      "s_waitcnt lgkmcnt(15)\n"
      "v_mfma_f32_32x32x16_bf16 v[32:47], v[80:83], v[232:235], v[32:47]\n"
      "s_add_u32 m0, s53, 32768\n"
      "s_add_u32 s97, s34, s35\n"
      "s_setprio 0\n"
      "buffer_load_dwordx4 v5, s[60:63], s97 offen sc1 lds\n"
      "s_setprio 1\n"
      "s_add_u32 s34, s34, 8192\n"
      "s_cmp_eq_u32 s34, 81920\n"
      "s_cselect_b32 s34, 0, s34\n"
      "v_mfma_f32_32x32x16_bf16 v[48:63], v[84:87], v[232:235], v[48:63]\n"
      "ds_read_b64_tr_b16 v[80:81], v3 offset:10240\n"
      "ds_read_b64_tr_b16 v[82:83], v3 offset:10752\n"
      "ds_read_b64_tr_b16 v[84:85], v4 offset:10240\n"
      "ds_read_b64_tr_b16 v[86:87], v4 offset:10752\n"
      "s_waitcnt lgkmcnt(12)\n"
      "v_mfma_f32_32x32x16_bf16 v[32:47], v[88:91], v[236:239], v[32:47]\n"
      "v_mfma_f32_32x32x16_bf16 v[48:63], v[92:95], v[236:239], v[48:63]\n"
      "ds_read_b64_tr_b16 v[88:89], v3 offset:11264\n"
      "ds_read_b64_tr_b16 v[90:91], v3 offset:11776\n"
      "ds_read_b64_tr_b16 v[92:93], v4 offset:11264\n"
      "ds_read_b64_tr_b16 v[94:95], v4 offset:11776\n"
      "s_waitcnt lgkmcnt(14)\n"
      "v_mfma_f32_32x32x16_bf16 v[32:47], v[64:67], v[240:243], v[32:47]\n"
      "v_and_b32 v6, 15, v0\n"
      "v_bfe_u32 v7, v0, 4, 1\n"
      "v_lshlrev_b32 v7, 10, v7\n"
      "v_lshl_add_u32 v6, v6, 4, v7\n"
      "v_lshrrev_b32 v7, 5, v0\n"
      "v_lshl_add_u32 v1, v7, 8, v6\n"
      "s_mov_b64 s[44:45], s[60:61]\n"
      "s_cmp_lt_u32 s54, 6\n"
      "s_cselect_b32 s46, 23068672, 0\n"
      "s_mul_i32 s10, s64, 3072\n"
      "s_add_u32 s10, s10, 40960\n"
      "s_waitcnt lgkmcnt(12)\n"
      "v_mfma_f32_32x32x16_bf16 v[48:63], v[68:71], v[240:243], v[48:63]\n"
      "s_waitcnt lgkmcnt(10)\n"
      "v_mfma_f32_32x32x16_bf16 v[32:47], v[72:75], v[244:247], v[32:47]\n"
      "s_waitcnt lgkmcnt(8)\n"
      "v_mfma_f32_32x32x16_bf16 v[48:63], v[76:79], v[244:247], v[48:63]\n"
      "s_waitcnt vmcnt(2)\n"
      "s_waitcnt vmcnt(1)\n"
      "s_cmp_lg_u32 s64, 7\n"
      "s_cbranch_scc1 Lacd_8\n"
      "s_waitcnt vmcnt(1)\n"
      "s_mov_b32 s96, 20000\n"
      "Lac_7:\n"
      "v_xor_b32 v18, s26, v18\n"
      "v_xor_b32 v19, s26, v19\n"
      "v_xor_b32 v20, s26, v20\n"
      "v_xor_b32 v21, s26, v21\n"
      "v_or3_b32 v17, v18, v19, v20\n"
      "v_or_b32 v17, v17, v21\n"
      "v_cmp_ne_u32 vcc, 0, v17\n"
      "s_cmp_eq_u64 vcc, 0\n"
      "s_cbranch_scc1 Lacd_8\n"
      "s_sub_u32 s96, s96, 1\n"
      "s_cmp_eq_u32 s96, 0\n"
      "s_cbranch_scc1 Lacd_8\n"
      "s_sleep 4\n"
      "buffer_load_dwordx4 v[18:21], v16, s[60:63], s33 offen sc1\n"
      "s_waitcnt vmcnt(0)\n"
      "s_branch Lac_7\n"
      "Lacd_8:\n"
      "s_barrier\n"
      "ds_read_b128 v[16:19], v5 offset:16384\n"
      "ds_read_b128 v[20:23], v5 offset:17408\n"
      "ds_read_b128 v[24:27], v5 offset:18432\n"
      "ds_read_b128 v[28:31], v5 offset:19456\n"
      "s_waitcnt lgkmcnt(10)\n"
      "v_mfma_f32_32x32x16_bf16 v[32:47], v[80:83], v[248:251], v[32:47]\n"
      "s_add_u32 m0, s53, 40960\n"
      "s_add_u32 s97, s34, s35\n"
      "s_setprio 0\n"
      "buffer_load_dwordx4 v5, s[60:63], s97 offen sc1 lds\n"
      "s_setprio 1\n"
      "s_add_u32 s34, s34, 8192\n"
      "s_cmp_eq_u32 s34, 81920\n"
      "s_cselect_b32 s34, 0, s34\n"
      "s_waitcnt lgkmcnt(8)\n"
      "v_mfma_f32_32x32x16_bf16 v[48:63], v[84:87], v[248:251], v[48:63]\n"
      "s_waitcnt lgkmcnt(6)\n"
      "v_mfma_f32_32x32x16_bf16 v[32:47], v[88:91], v[252:255], v[32:47]\n"
      "s_waitcnt lgkmcnt(4)\n"
      "v_mfma_f32_32x32x16_bf16 v[48:63], v[92:95], v[252:255], v[48:63]\n"
      "s_waitcnt lgkmcnt(3)\n"
      "v_mfma_f32_32x32x16_bf16 v[64:79], v[16:19], v[96:99], 0\n"
      "s_mov_b32 s8, 0xbfb8aa3b\n"
      "s_nop 6\n"
      "v_mul_f32 v8, s8, v32\n"
      "v_mul_f32 v9, s8, v33\n"
      "v_mul_f32 v10, s8, v34\n"
      "v_mul_f32 v11, s8, v35\n"
      "v_mul_f32 v12, s8, v36\n"
      "v_mul_f32 v13, s8, v37\n"
      "s_waitcnt lgkmcnt(2)\n"
      "v_mfma_f32_32x32x16_bf16 v[80:95], v[20:23], v[96:99], 0\n"
      "ds_read_b128 v[16:19], v5 offset:20480\n"
      "ds_read_b128 v[20:23], v5 offset:21504\n"
      "v_mul_f32 v14, s8, v38\n"
      "v_mul_f32 v15, s8, v39\n"
      "v_exp_f32 v8, v8\n"
      "v_exp_f32 v9, v9\n"
      "v_exp_f32 v10, v10\n"
      "s_waitcnt lgkmcnt(3)\n"
      "v_mfma_f32_32x32x16_bf16 v[64:79], v[24:27], v[100:103], v[64:79]\n"
      "v_exp_f32 v11, v11\n"
      "v_exp_f32 v12, v12\n"
      "v_exp_f32 v13, v13\n"
      "v_exp_f32 v14, v14\n"
      "s_waitcnt lgkmcnt(2)\n"
      "v_mfma_f32_32x32x16_bf16 v[80:95], v[28:31], v[100:103], v[80:95]\n"
      "ds_read_b128 v[24:27], v5 offset:22528\n"
      "ds_read_b128 v[28:31], v5 offset:23552\n"
      "ds_read_b128 v[96:99], v5 offset:24576\n"
      "ds_read_b128 v[100:103], v5 offset:25600\n"
      "v_exp_f32 v15, v15\n"
      "v_add_f32 v8, 1.0, v8\n"
      "v_add_f32 v9, 1.0, v9\n"
      "v_add_f32 v10, 1.0, v10\n"
      "v_add_f32 v11, 1.0, v11\n"
      "v_add_f32 v12, 1.0, v12\n"
      "s_waitcnt lgkmcnt(5)\n"
      "v_mfma_f32_32x32x16_bf16 v[64:79], v[16:19], v[104:107], v[64:79]\n"
      "v_add_f32 v13, 1.0, v13\n"
      "v_add_f32 v14, 1.0, v14\n"
      "v_add_f32 v15, 1.0, v15\n"
      "v_rcp_f32 v8, v8\n"
      "v_rcp_f32 v9, v9\n"
      "s_waitcnt lgkmcnt(4)\n"
      "v_mfma_f32_32x32x16_bf16 v[80:95], v[20:23], v[104:107], v[80:95]\n"
      "ds_read_b128 v[16:19], v5 offset:26624\n"
      "ds_read_b128 v[20:23], v5 offset:27648\n"
      "s_add_u32 m0, s53, 49152\n"
      "s_add_u32 s97, s34, s35\n"
      "s_setprio 0\n"
      "buffer_load_dwordx4 v5, s[60:63], s97 offen sc1 lds\n"
      "s_setprio 1\n"
      "s_add_u32 s34, s34, 8192\n"
      "s_cmp_eq_u32 s34, 81920\n"
      "s_cselect_b32 s34, 0, s34\n"
      "v_rcp_f32 v10, v10\n"
      "v_rcp_f32 v11, v11\n"
      "v_rcp_f32 v12, v12\n"
      "v_rcp_f32 v13, v13\n"
      "s_waitcnt lgkmcnt(5)\n"
      "v_mfma_f32_32x32x16_bf16 v[64:79], v[24:27], v[108:111], v[64:79]\n"
      "v_rcp_f32 v14, v14\n"
      "v_rcp_f32 v15, v15\n"
      "v_mul_f32 v8, v8, v32\n"
      "v_mul_f32 v9, v9, v33\n"
      "v_mul_f32 v10, v10, v34\n"
      "s_waitcnt lgkmcnt(4)\n"
      "v_mfma_f32_32x32x16_bf16 v[80:95], v[28:31], v[108:111], v[80:95]\n"
      "ds_read_b128 v[24:27], v5 offset:28672\n"
      "ds_read_b128 v[28:31], v5 offset:29696\n"
      "ds_read_b128 v[104:107], v5 offset:30720\n"
      "ds_read_b128 v[108:111], v5 offset:31744\n"
      "v_mul_f32 v11, v11, v35\n"
      "v_mul_f32 v12, v12, v36\n"
      "v_mul_f32 v13, v13, v37\n"
      "v_mul_f32 v14, v14, v38\n"
      "v_mul_f32 v15, v15, v39\n"
      "v_cvt_pk_bf16_f32 v32, v8, v9\n"
      "v_cvt_pk_bf16_f32 v33, v10, v11\n"
      "s_waitcnt lgkmcnt(7)\n"
      "v_mfma_f32_32x32x16_bf16 v[64:79], v[96:99], v[112:115], v[64:79]\n"
      "v_cvt_pk_bf16_f32 v34, v12, v13\n"
      "v_cvt_pk_bf16_f32 v35, v14, v15\n"
      "s_setprio 0\n"
      "buffer_store_dwordx4 v[32:35], v1, s[60:63], s20 offen offset:0 sc1\n"
      "s_setprio 1\n"
      "v_mul_f32 v8, s8, v40\n"
      "v_mul_f32 v9, s8, v41\n"
      "v_mul_f32 v10, s8, v42\n"
      "v_mul_f32 v11, s8, v43\n"
      "s_waitcnt lgkmcnt(6)\n"
      "v_mfma_f32_32x32x16_bf16 v[80:95], v[100:103], v[112:115], v[80:95]\n"
      "s_add_u32 m0, s27, 0\n"
      "s_add_u32 s97, s79, 163840\n"
      "s_setprio 0\n"
      "buffer_load_dwordx4 v5, s[60:63], s97 offen sc1 lds\n"
      "s_setprio 1\n"
      "v_mul_f32 v12, s8, v44\n"
      "v_mul_f32 v13, s8, v45\n"
      "v_mul_f32 v14, s8, v46\n"
      "v_mul_f32 v15, s8, v47\n"
      "v_exp_f32 v8, v8\n"
      "v_exp_f32 v9, v9\n"
      "s_waitcnt lgkmcnt(5)\n"
      "v_mfma_f32_32x32x16_bf16 v[64:79], v[16:19], v[116:119], v[64:79]\n"
      "v_exp_f32 v10, v10\n"
      "v_exp_f32 v11, v11\n"
      "v_exp_f32 v12, v12\n"
      "v_exp_f32 v13, v13\n"
      "s_waitcnt lgkmcnt(4)\n"
      "v_mfma_f32_32x32x16_bf16 v[80:95], v[20:23], v[116:119], v[80:95]\n"
      "s_add_u32 m0, s27, 0\n"
      "s_add_u32 s97, s79, 163840\n"
      "s_setprio 0\n"
      "buffer_load_dwordx4 v5, s[60:63], s97 offen offset:1024 sc1 lds\n"
      "s_setprio 1\n"
      "v_exp_f32 v14, v14\n"
      "v_exp_f32 v15, v15\n"
      "v_add_f32 v8, 1.0, v8\n"
      "v_add_f32 v9, 1.0, v9\n"
      "v_add_f32 v10, 1.0, v10\n"
      "s_waitcnt vmcnt(5)\n"
      "s_waitcnt vmcnt(4)\n"
      "s_barrier\n"
      "ds_read_b128 v[96:99], v5 offset:32768\n"
      "ds_read_b128 v[100:103], v5 offset:33792\n"
      "ds_read_b128 v[112:115], v5 offset:34816\n"
      "ds_read_b128 v[116:119], v5 offset:35840\n"
      "s_waitcnt lgkmcnt(7)\n"
      "v_mfma_f32_32x32x16_bf16 v[64:79], v[24:27], v[120:123], v[64:79]\n"
      "v_add_f32 v11, 1.0, v11\n"
      "v_add_f32 v12, 1.0, v12\n"
      "v_add_f32 v13, 1.0, v13\n"
      "v_add_f32 v14, 1.0, v14\n"
      "v_add_f32 v15, 1.0, v15\n"
      "v_rcp_f32 v8, v8\n"
      "s_waitcnt lgkmcnt(6)\n"
      "v_mfma_f32_32x32x16_bf16 v[80:95], v[28:31], v[120:123], v[80:95]\n"
      "s_add_u32 m0, s27, 0\n"
      "s_add_u32 s97, s79, 163840\n"
      "s_setprio 0\n"
      "buffer_load_dwordx4 v5, s[60:63], s97 offen offset:2048 sc1 lds\n"
      "s_setprio 1\n"
      "v_rcp_f32 v9, v9\n"
      "v_rcp_f32 v10, v10\n"
      "v_rcp_f32 v11, v11\n"
      "v_rcp_f32 v12, v12\n"
      "s_waitcnt lgkmcnt(5)\n"
      "v_mfma_f32_32x32x16_bf16 v[64:79], v[104:107], v[124:127], v[64:79]\n"
      "v_rcp_f32 v13, v13\n"
      "v_rcp_f32 v14, v14\n"
      "v_rcp_f32 v15, v15\n"
      "v_mul_f32 v8, v8, v40\n"
      "s_waitcnt lgkmcnt(4)\n"
      "v_mfma_f32_32x32x16_bf16 v[80:95], v[108:111], v[124:127], v[80:95]\n"
      "ds_read_b128 v[104:107], v5 offset:36864\n"
      "ds_read_b128 v[108:111], v5 offset:37888\n"
      "ds_read_b128 v[120:123], v5 offset:38912\n"
      "ds_read_b128 v[124:127], v5 offset:39936\n"
      "s_add_u32 m0, s27, 0\n"
      "s_add_u32 s97, s79, 163840\n"
      "s_setprio 0\n"
      "buffer_load_dwordx4 v5, s[60:63], s97 offen offset:3072 sc1 lds\n"
      "s_setprio 1\n"
      "v_mul_f32 v9, v9, v41\n"
      "v_mul_f32 v10, v10, v42\n"
      "v_mul_f32 v11, v11, v43\n"
      "v_mul_f32 v12, v12, v44\n"
      "v_mul_f32 v13, v13, v45\n"
      "v_mul_f32 v14, v14, v46\n"
      "v_mul_f32 v15, v15, v47\n"
      "s_waitcnt lgkmcnt(7)\n"
      "v_mfma_f32_32x32x16_bf16 v[64:79], v[96:99], v[128:131], v[64:79]\n"
      "v_cvt_pk_bf16_f32 v40, v8, v9\n"
      "v_cvt_pk_bf16_f32 v41, v10, v11\n"
      "v_cvt_pk_bf16_f32 v42, v12, v13\n"
      "v_cvt_pk_bf16_f32 v43, v14, v15\n"
      "s_setprio 0\n"
      "buffer_store_dwordx4 v[40:43], v1, s[60:63], s20 offen offset:512 sc1\n"
      "s_setprio 1\n"
      "v_mul_f32 v8, s8, v48\n"
      "v_mul_f32 v9, s8, v49\n"
      "s_waitcnt lgkmcnt(6)\n"
      "v_mfma_f32_32x32x16_bf16 v[80:95], v[100:103], v[128:131], v[80:95]\n"
      "ds_read_b128 v[96:99], v5 offset:40960\n"
      "ds_read_b128 v[100:103], v5 offset:41984\n"
      "s_add_u32 m0, s27, 4096\n"
      "s_add_u32 s97, s79, 167936\n"
      "s_setprio 0\n"
      "buffer_load_dwordx4 v5, s[60:63], s97 offen sc1 lds\n"
      "s_setprio 1\n"
      "v_mul_f32 v10, s8, v50\n"
      "v_mul_f32 v11, s8, v51\n"
      "v_mul_f32 v12, s8, v52\n"
      "v_mul_f32 v13, s8, v53\n"
      "v_mul_f32 v14, s8, v54\n"
      "v_mul_f32 v15, s8, v55\n"
      "v_exp_f32 v8, v8\n"
      "s_waitcnt lgkmcnt(7)\n"
      "v_mfma_f32_32x32x16_bf16 v[64:79], v[112:115], v[132:135], v[64:79]\n"
      "v_exp_f32 v9, v9\n"
      "v_exp_f32 v10, v10\n"
      "v_exp_f32 v11, v11\n"
      "v_exp_f32 v12, v12\n"
      "s_waitcnt lgkmcnt(6)\n"
      "v_mfma_f32_32x32x16_bf16 v[80:95], v[116:119], v[132:135], v[80:95]\n"
      "ds_read_b128 v[112:115], v5 offset:43008\n"
      "ds_read_b128 v[116:119], v5 offset:44032\n"
      "s_add_u32 m0, s53, 57344\n"
      "s_add_u32 s97, s34, s35\n"
      "s_setprio 0\n"
      "buffer_load_dwordx4 v5, s[60:63], s97 offen sc1 lds\n"
      "s_setprio 1\n"
      "s_add_u32 s34, s34, 8192\n"
      "s_cmp_eq_u32 s34, 81920\n"
      "s_cselect_b32 s34, 0, s34\n"
      "v_exp_f32 v13, v13\n"
      "v_exp_f32 v14, v14\n"
      "v_exp_f32 v15, v15\n"
      "v_add_f32 v8, 1.0, v8\n"
      "s_waitcnt lgkmcnt(7)\n"
      "v_mfma_f32_32x32x16_bf16 v[64:79], v[104:107], v[136:139], v[64:79]\n"
      "v_add_f32 v9, 1.0, v9\n"
      "v_add_f32 v10, 1.0, v10\n"
      "v_add_f32 v11, 1.0, v11\n"
      "v_add_f32 v12, 1.0, v12\n"
      "v_add_f32 v13, 1.0, v13\n"
      "v_add_f32 v14, 1.0, v14\n"
      "v_add_f32 v15, 1.0, v15\n"
      "s_waitcnt lgkmcnt(6)\n"
      "v_mfma_f32_32x32x16_bf16 v[80:95], v[108:111], v[136:139], v[80:95]\n"
      "ds_read_b128 v[104:107], v5 offset:45056\n"
      "ds_read_b128 v[108:111], v5 offset:46080\n"
      "s_add_u32 m0, s53, 0\n"
      "s_add_u32 s97, s34, s35\n"
      "s_setprio 0\n"
      "buffer_load_dwordx4 v5, s[60:63], s97 offen sc1 lds\n"
      "s_setprio 1\n"
      "s_add_u32 s34, s34, 8192\n"
      "s_cmp_eq_u32 s34, 81920\n"
      "s_cselect_b32 s34, 0, s34\n"
      "v_rcp_f32 v8, v8\n"
      "v_rcp_f32 v9, v9\n"
      "v_rcp_f32 v10, v10\n"
      "v_rcp_f32 v11, v11\n"
      "s_waitcnt lgkmcnt(7)\n"
      "v_mfma_f32_32x32x16_bf16 v[64:79], v[120:123], v[140:143], v[64:79]\n"
      "v_rcp_f32 v12, v12\n"
      "v_rcp_f32 v13, v13\n"
      "v_rcp_f32 v14, v14\n"
      "v_rcp_f32 v15, v15\n"
      "s_waitcnt lgkmcnt(6)\n"
      "v_mfma_f32_32x32x16_bf16 v[80:95], v[124:127], v[140:143], v[80:95]\n"
      "ds_read_b128 v[120:123], v5 offset:47104\n"
      "ds_read_b128 v[124:127], v5 offset:48128\n"
      "v_mul_f32 v8, v8, v48\n"
      "v_mul_f32 v9, v9, v49\n"
      "v_mul_f32 v10, v10, v50\n"
      "v_mul_f32 v11, v11, v51\n"
      "v_mul_f32 v12, v12, v52\n"
      "v_mul_f32 v13, v13, v53\n"
      "v_mul_f32 v14, v14, v54\n"
      "s_waitcnt lgkmcnt(7)\n"
      "v_mfma_f32_32x32x16_bf16 v[64:79], v[96:99], v[144:147], v[64:79]\n"
      "v_mul_f32 v15, v15, v55\n"
      "v_cvt_pk_bf16_f32 v48, v8, v9\n"
      "v_cvt_pk_bf16_f32 v49, v10, v11\n"
      "v_cvt_pk_bf16_f32 v50, v12, v13\n"
      "v_cvt_pk_bf16_f32 v51, v14, v15\n"
      "s_add_u32 s20, s20, 4096\n"
      "s_setprio 0\n"
      "buffer_store_dwordx4 v[48:51], v1, s[60:63], s20 offen offset:0 sc1\n"
      "s_setprio 1\n"
      "s_waitcnt lgkmcnt(6)\n"
      "v_mfma_f32_32x32x16_bf16 v[80:95], v[100:103], v[144:147], v[80:95]\n"
      "v_mul_f32 v8, s8, v56\n"
      "v_mul_f32 v9, s8, v57\n"
      "v_mul_f32 v10, s8, v58\n"
      "v_mul_f32 v11, s8, v59\n"
      "v_mul_f32 v12, s8, v60\n"
      "v_mul_f32 v13, s8, v61\n"
      "v_mul_f32 v14, s8, v62\n"
      "s_waitcnt lgkmcnt(5)\n"
      "v_mfma_f32_32x32x16_bf16 v[64:79], v[112:115], v[148:151], v[64:79]\n"
      "v_mul_f32 v15, s8, v63\n"
      "v_exp_f32 v8, v8\n"
      "v_exp_f32 v9, v9\n"
      "v_exp_f32 v10, v10\n"
      "s_waitcnt lgkmcnt(4)\n"
      "v_mfma_f32_32x32x16_bf16 v[80:95], v[116:119], v[148:151], v[80:95]\n"
      "v_exp_f32 v11, v11\n"
      "v_exp_f32 v12, v12\n"
      "v_exp_f32 v13, v13\n"
      "v_exp_f32 v14, v14\n"
      "s_waitcnt vmcnt(10)\n"
      "s_waitcnt vmcnt(2)\n"
      "s_barrier\n"
      "ds_read_b128 v[96:99], v5 offset:49152\n"
      "ds_read_b128 v[100:103], v5 offset:50176\n"
      "ds_read_b128 v[112:115], v5 offset:51200\n"
      "ds_read_b128 v[116:119], v5 offset:52224\n"
      "s_waitcnt lgkmcnt(7)\n"
      "v_mfma_f32_32x32x16_bf16 v[64:79], v[104:107], v[152:155], v[64:79]\n"
      "v_exp_f32 v15, v15\n"
      "v_add_f32 v8, 1.0, v8\n"
      "v_add_f32 v9, 1.0, v9\n"
      "v_add_f32 v10, 1.0, v10\n"
      "v_add_f32 v11, 1.0, v11\n"
      "v_add_f32 v12, 1.0, v12\n"
      "s_waitcnt lgkmcnt(6)\n"
      "v_mfma_f32_32x32x16_bf16 v[80:95], v[108:111], v[152:155], v[80:95]\n"
      "ds_read_b128 v[104:107], v5 offset:53248\n"
      "ds_read_b128 v[108:111], v5 offset:54272\n"
      "s_add_u32 m0, s53, 8192\n"
      "s_add_u32 s97, s34, s35\n"
      "s_setprio 0\n"
      "buffer_load_dwordx4 v5, s[60:63], s97 offen sc1 lds\n"
      "s_setprio 1\n"
      "s_add_u32 s34, s34, 8192\n"
      "s_cmp_eq_u32 s34, 81920\n"
      "s_cselect_b32 s34, 0, s34\n"
      "v_add_f32 v13, 1.0, v13\n"
      "v_add_f32 v14, 1.0, v14\n"
      "v_add_f32 v15, 1.0, v15\n"
      "v_rcp_f32 v8, v8\n"
      "v_rcp_f32 v9, v9\n"
      "s_waitcnt lgkmcnt(7)\n"
      "v_mfma_f32_32x32x16_bf16 v[64:79], v[120:123], v[156:159], v[64:79]\n"
      "v_rcp_f32 v10, v10\n"
      "v_rcp_f32 v11, v11\n"
      "v_rcp_f32 v12, v12\n"
      "v_rcp_f32 v13, v13\n"
      "s_waitcnt lgkmcnt(6)\n"
      "v_mfma_f32_32x32x16_bf16 v[80:95], v[124:127], v[156:159], v[80:95]\n"
      "ds_read_b128 v[120:123], v5 offset:55296\n"
      "ds_read_b128 v[124:127], v5 offset:56320\n"
      "s_add_u32 m0, s53, 16384\n"
      "s_add_u32 s97, s34, s35\n"
      "s_setprio 0\n"
      "buffer_load_dwordx4 v5, s[60:63], s97 offen sc1 lds\n"
      "s_setprio 1\n"
      "s_add_u32 s34, s34, 8192\n"
      "s_cmp_eq_u32 s34, 81920\n"
      "s_cselect_b32 s34, 0, s34\n"
      "v_rcp_f32 v14, v14\n"
      "v_rcp_f32 v15, v15\n"
      "v_mul_f32 v8, v8, v56\n"
      "v_mul_f32 v9, v9, v57\n"
      "v_mul_f32 v10, v10, v58\n"
      "s_waitcnt lgkmcnt(7)\n"
      "v_mfma_f32_32x32x16_bf16 v[64:79], v[96:99], v[160:163], v[64:79]\n"
      "v_mul_f32 v11, v11, v59\n"
      "v_mul_f32 v12, v12, v60\n"
      "v_mul_f32 v13, v13, v61\n"
      "v_mul_f32 v14, v14, v62\n"
      "v_mul_f32 v15, v15, v63\n"
      "v_cvt_pk_bf16_f32 v56, v8, v9\n"
      "v_cvt_pk_bf16_f32 v57, v10, v11\n"
      "s_waitcnt lgkmcnt(6)\n"
      "v_mfma_f32_32x32x16_bf16 v[80:95], v[100:103], v[160:163], v[80:95]\n"
      "ds_read_b128 v[96:99], v5 offset:57344\n"
      "ds_read_b128 v[100:103], v5 offset:58368\n"
      "s_add_u32 m0, s53, 24576\n"
      "s_add_u32 s97, s34, s35\n"
      "s_setprio 0\n"
      "buffer_load_dwordx4 v5, s[60:63], s97 offen sc1 lds\n"
      "s_setprio 1\n"
      "s_add_u32 s34, s34, 8192\n"
      "s_cmp_eq_u32 s34, 81920\n"
      "s_cselect_b32 s34, 0, s34\n"
      "v_cvt_pk_bf16_f32 v58, v12, v13\n"
      "v_cvt_pk_bf16_f32 v59, v14, v15\n"
      "s_setprio 0\n"
      "buffer_store_dwordx4 v[56:59], v1, s[60:63], s20 offen offset:512 sc1\n"
      "s_setprio 1\n"
      "v_and_b32 v6, 15, v0\n"
      "v_bfe_u32 v7, v0, 4, 1\n"
      "v_lshlrev_b32 v7, 10, v7\n"
      "v_lshl_add_u32 v23, v6, 3, v7\n"
      "s_waitcnt lgkmcnt(7)\n"
      "v_mfma_f32_32x32x16_bf16 v[64:79], v[112:115], v[164:167], v[64:79]\n"
      "s_lshl_b32 s73, s70, 6\n"
      "v_add_lshl_u32 v6, v6, s73, 2\n"
      "v_add_u32 v30, 38720, v6\n"
      "v_and_b32 v6, 31, v0\n"
      "s_lshl_b32 s73, s64, 5\n"
      "v_add_lshl_u32 v7, v6, s73, 2\n"
      "v_add_u32 v27, 38720, v7\n"
      "v_mul_u32_u24 v6, 144, v6\n"
      "s_mul_i32 s73, s64, 4608\n"
      "v_add_u32 v16, s73, v6\n"
      "s_waitcnt lgkmcnt(6)\n"
      "v_mfma_f32_32x32x16_bf16 v[80:95], v[116:119], v[164:167], v[80:95]\n"
      "ds_read_b128 v[112:115], v5 offset:59392\n"
      "ds_read_b128 v[116:119], v5 offset:60416\n"
      "s_add_u32 s97, s79, 0\n"
      "s_setprio 0\n"
      "buffer_load_dwordx4 v[128:131], v5, s[60:63], s97 offen sc1\n"
      "s_setprio 1\n"
      "v_lshrrev_b32 v31, 5, v0\n"
      "v_mul_u32_u24 v31, 24, v31\n"
      "v_add_u32 v17, v31, v16\n"
      "v_lshrrev_b32 v6, 4, v0\n"
      "v_bfe_u32 v7, v0, 2, 2\n"
      "v_lshl_add_u32 v7, v6, 3, v7\n"
      "v_mul_u32_u24 v7, 144, v7\n"
      "s_waitcnt lgkmcnt(7)\n"
      "v_mfma_f32_32x32x16_bf16 v[64:79], v[104:107], v[168:171], v[64:79]\n"
      "v_and_b32 v31, 3, v0\n"
      "v_lshl_add_u32 v18, v31, 3, v7\n"
      "s_lshl_b32 s73, s64, 2\n"
      "v_add_u32 v6, s73, v6\n"
      "v_and_b32 v7, 15, v0\n"
      "v_cmp_gt_u32 s[16:17], 10, v6\n"
      "v_cmp_gt_u32 vcc, 6, v7\n"
      "s_and_b64 s[18:19], vcc, s[16:17]\n"
      "v_mul_u32_u24 v6, 22, v6\n"
      "v_add_lshl_u32 v19, v6, v7, 4\n"
      "s_waitcnt lgkmcnt(6)\n"
      "v_mfma_f32_32x32x16_bf16 v[80:95], v[108:111], v[168:171], v[80:95]\n"
      "ds_read_b128 v[104:107], v5 offset:61440\n"
      "ds_read_b128 v[108:111], v5 offset:62464\n"
      "s_add_u32 s97, s79, 0\n"
      "s_setprio 0\n"
      "buffer_load_dwordx4 v[132:135], v5, s[60:63], s97 offen offset:1024 sc1\n"
      "s_setprio 1\n"
      "s_lshl_b32 s73, s64, 6\n"
      "v_add_u32 v31, s73, v0\n"
      "v_min_u32 v31, 219, v31\n"
      "v_lshlrev_b32 v29, 4, v31\n"
      "v_mul_u32_u24 v6, 2979, v31\n"
      "v_lshrrev_b32 v6, 16, v6\n"
      "v_mul_u32_u24 v7, 22, v6\n"
      "v_sub_u32 v7, v31, v7\n"
      "s_waitcnt lgkmcnt(7)\n"
      "v_mfma_f32_32x32x16_bf16 v[64:79], v[120:123], v[172:175], v[64:79]\n"
      "v_mul_u32_u24 v7, 80, v7\n"
      "v_lshl_add_u32 v6, v6, 3, v7\n"
      "v_add_u32 v21, 36864, v6\n"
      "v_bfe_u32 v6, v0, 3, 2\n"
      "v_bfe_u32 v7, v0, 2, 1\n"
      "v_and_b32 v31, 3, v0\n"
      "v_lshlrev_b32 v7, 3, v7\n"
      "s_waitcnt lgkmcnt(6)\n"
      "v_mfma_f32_32x32x16_bf16 v[80:95], v[124:127], v[172:175], v[80:95]\n"
      "ds_read_b128 v[120:123], v5 offset:63488\n"
      "ds_read_b128 v[124:127], v5 offset:64512\n"
      "s_add_u32 s97, s79, 0\n"
      "s_setprio 0\n"
      "buffer_load_dwordx4 v[136:139], v5, s[60:63], s97 offen offset:2048 sc1\n"
      "s_setprio 1\n"
      "v_lshl_add_u32 v7, v6, 2, v7\n"
      "v_cmp_eq_u32 vcc, 2, v6\n"
      "s_nop 1\n"
      "v_cndmask_b32 v7, v7, 16, vcc\n"
      "v_add_u32 v7, v7, v31\n"
      "v_cmp_le_u32 vcc, 10, v7\n"
      "s_nop 1\n"
      "v_cndmask_b32 v31, 0, 2, vcc\n"
      "v_add_u32 v7, v7, v31\n"
      "v_cmp_eq_u32 vcc, 3, v6\n"
      "s_nop 1\n"
      "v_cndmask_b32 v7, v7, 11, vcc\n"
      "v_cmp_le_u32 vcc, 32, v0\n"
      "s_nop 1\n"
      "v_cndmask_b32 v7, v7, 22, vcc\n"
      "s_waitcnt lgkmcnt(7)\n"
      "v_mfma_f32_32x32x16_bf16 v[64:79], v[96:99], v[176:179], v[64:79]\n"
      "v_mul_u32_u24 v7, 80, v7\n"
      "v_add_u32 v22, 36864, v7\n"
      "v_lshrrev_b32 v6, 4, v0\n"
      "v_lshlrev_b32 v6, 6, v6\n"
      "v_xor_b32 v6, v6, v5\n"
      "s_mul_i32 s73, s64, 20480\n"
      "v_add_u32 v24, s73, v6\n"
      "v_lshrrev_b32 v7, 2, v0\n"
      "s_waitcnt lgkmcnt(6)\n"
      "v_mfma_f32_32x32x16_bf16 v[80:95], v[100:103], v[176:179], v[80:95]\n"
      "s_add_u32 s97, s79, 0\n"
      "s_setprio 0\n"
      "buffer_load_dwordx4 v[140:143], v5, s[60:63], s97 offen offset:3072 sc1\n"
      "s_setprio 1\n"
      "v_and_b32 v31, 3, v0\n"
      "v_lshlrev_b32 v6, 2, v31\n"
      "v_xor_b32 v6, v6, v7\n"
      "v_lshl_add_u32 v6, v31, 4, v6\n"
      "v_lshlrev_b32 v6, 4, v6\n"
      "v_add_u32 v25, s53, v6\n"
      "v_mul_u32_u24 v7, 1280, v7\n"
      "s_waitcnt lgkmcnt(5)\n"
      "v_mfma_f32_32x32x16_bf16 v[64:79], v[112:115], v[180:183], v[64:79]\n"
      "v_lshl_add_u32 v26, v31, 3, v7\n"
      "v_add_u32 v20, s27, v5\n"
      "s_waitcnt lgkmcnt(4)\n"
      "v_mfma_f32_32x32x16_bf16 v[80:95], v[116:119], v[180:183], v[80:95]\n"
      "s_add_u32 s97, s79, 4096\n"
      "s_setprio 0\n"
      "buffer_load_dwordx4 v[144:147], v5, s[60:63], s97 offen sc1\n"
      "s_setprio 1\n"
      "s_waitcnt vmcnt(5)\n"
      "s_mov_b64 exec, 1\n"
      "buffer_store_dword v2, off, s[60:63], s21 sc1\n"
      "s_mov_b64 exec, -1\n"
      "s_barrier\n"
      "ds_read_b128 v[96:99], v5 offset:0\n"
      "ds_read_b128 v[100:103], v5 offset:1024\n"
      "ds_read_b128 v[112:115], v5 offset:2048\n"
      "ds_read_b128 v[116:119], v5 offset:3072\n"
      "s_waitcnt lgkmcnt(7)\n"
      "v_mfma_f32_32x32x16_bf16 v[64:79], v[104:107], v[184:187], v[64:79]\n"
      "s_waitcnt lgkmcnt(6)\n"
      "v_mfma_f32_32x32x16_bf16 v[80:95], v[108:111], v[184:187], v[80:95]\n"
      "ds_read_b128 v[104:107], v5 offset:4096\n"
      "ds_read_b128 v[108:111], v5 offset:5120\n"
      "s_add_u32 s97, s79, 40960\n"
      "s_setprio 0\n"
      "buffer_load_dwordx4 v[148:151], v5, s[60:63], s97 offen sc1\n"
      "s_setprio 1\n"
      "s_waitcnt lgkmcnt(7)\n"
      "v_mfma_f32_32x32x16_bf16 v[64:79], v[120:123], v[188:191], v[64:79]\n"
      "s_waitcnt lgkmcnt(6)\n"
      "v_mfma_f32_32x32x16_bf16 v[80:95], v[124:127], v[188:191], v[80:95]\n"
      "ds_read_b128 v[120:123], v5 offset:6144\n"
      "ds_read_b128 v[124:127], v5 offset:7168\n"
      "s_add_u32 s97, s79, 40960\n"
      "s_setprio 0\n"
      "buffer_load_dwordx4 v[152:155], v5, s[60:63], s97 offen offset:1024 sc1\n"
      "s_setprio 1\n"
      "s_waitcnt lgkmcnt(7)\n"
      "v_mfma_f32_32x32x16_bf16 v[64:79], v[96:99], v[192:195], v[64:79]\n"
      "s_waitcnt lgkmcnt(6)\n"
      "v_mfma_f32_32x32x16_bf16 v[80:95], v[100:103], v[192:195], v[80:95]\n"
      "ds_read_b128 v[96:99], v5 offset:8192\n"
      "ds_read_b128 v[100:103], v5 offset:9216\n"
      "s_add_u32 s97, s79, 40960\n"
      "s_setprio 0\n"
      "buffer_load_dwordx4 v[156:159], v5, s[60:63], s97 offen offset:2048 sc1\n"
      "s_setprio 1\n"
      "s_waitcnt lgkmcnt(7)\n"
      "v_mfma_f32_32x32x16_bf16 v[64:79], v[112:115], v[196:199], v[64:79]\n"
      "s_waitcnt lgkmcnt(6)\n"
      "v_mfma_f32_32x32x16_bf16 v[80:95], v[116:119], v[196:199], v[80:95]\n"
      "ds_read_b128 v[112:115], v5 offset:10240\n"
      "ds_read_b128 v[116:119], v5 offset:11264\n"
      "s_add_u32 s97, s79, 40960\n"
      "s_setprio 0\n"
      "buffer_load_dwordx4 v[160:163], v5, s[60:63], s97 offen offset:3072 sc1\n"
      "s_setprio 1\n"
      "s_waitcnt lgkmcnt(7)\n"
      "v_mfma_f32_32x32x16_bf16 v[64:79], v[104:107], v[200:203], v[64:79]\n"
      "s_waitcnt lgkmcnt(6)\n"
      "v_mfma_f32_32x32x16_bf16 v[80:95], v[108:111], v[200:203], v[80:95]\n"
      "ds_read_b128 v[104:107], v5 offset:12288\n"
      "ds_read_b128 v[108:111], v5 offset:13312\n"
      "s_add_u32 s97, s79, 45056\n"
      "s_setprio 0\n"
      "buffer_load_dwordx4 v[164:167], v5, s[60:63], s97 offen sc1\n"
      "s_setprio 1\n"
      "s_waitcnt lgkmcnt(7)\n"
      "v_mfma_f32_32x32x16_bf16 v[64:79], v[120:123], v[204:207], v[64:79]\n"
      "s_waitcnt lgkmcnt(6)\n"
      "v_mfma_f32_32x32x16_bf16 v[80:95], v[124:127], v[204:207], v[80:95]\n"
      "ds_read_b128 v[120:123], v5 offset:14336\n"
      "ds_read_b128 v[124:127], v5 offset:15360\n"
      "s_add_u32 s97, s79, 81920\n"
      "s_setprio 0\n"
      "buffer_load_dwordx4 v[168:171], v5, s[60:63], s97 offen sc1\n"
      "s_setprio 1\n"
      "s_waitcnt lgkmcnt(7)\n"
      "v_mfma_f32_32x32x16_bf16 v[64:79], v[96:99], v[208:211], v[64:79]\n"
      "s_waitcnt lgkmcnt(6)\n"
      "v_mfma_f32_32x32x16_bf16 v[80:95], v[100:103], v[208:211], v[80:95]\n"
      "ds_read_b128 v[96:99], v5 offset:16384\n"
      "ds_read_b128 v[100:103], v5 offset:17408\n"
      "s_add_u32 s97, s79, 81920\n"
      "s_setprio 0\n"
      "buffer_load_dwordx4 v[172:175], v5, s[60:63], s97 offen offset:1024 sc1\n"
      "s_setprio 1\n"
      "s_waitcnt lgkmcnt(7)\n"
      "v_mfma_f32_32x32x16_bf16 v[64:79], v[112:115], v[212:215], v[64:79]\n"
      "s_waitcnt lgkmcnt(6)\n"
      "v_mfma_f32_32x32x16_bf16 v[80:95], v[116:119], v[212:215], v[80:95]\n"
      "ds_read_b128 v[112:115], v5 offset:18432\n"
      "ds_read_b128 v[116:119], v5 offset:19456\n"
      "s_add_u32 s97, s79, 81920\n"
      "s_setprio 0\n"
      "buffer_load_dwordx4 v[176:179], v5, s[60:63], s97 offen offset:2048 sc1\n"
      "s_setprio 1\n"
      "s_waitcnt lgkmcnt(7)\n"
      "v_mfma_f32_32x32x16_bf16 v[64:79], v[104:107], v[216:219], v[64:79]\n"
      "s_waitcnt lgkmcnt(6)\n"
      "v_mfma_f32_32x32x16_bf16 v[80:95], v[108:111], v[216:219], v[80:95]\n"
      "ds_read_b128 v[104:107], v5 offset:20480\n"
      "ds_read_b128 v[108:111], v5 offset:21504\n"
      "s_add_u32 s97, s79, 81920\n"
      "s_setprio 0\n"
      "buffer_load_dwordx4 v[180:183], v5, s[60:63], s97 offen offset:3072 sc1\n"
      "s_setprio 1\n"
      "s_waitcnt lgkmcnt(7)\n"
      "v_mfma_f32_32x32x16_bf16 v[64:79], v[120:123], v[220:223], v[64:79]\n"
      "s_waitcnt lgkmcnt(6)\n"
      "v_mfma_f32_32x32x16_bf16 v[80:95], v[124:127], v[220:223], v[80:95]\n"
      "ds_read_b128 v[120:123], v5 offset:22528\n"
      "ds_read_b128 v[124:127], v5 offset:23552\n"
      "s_add_u32 s97, s79, 86016\n"
      "s_setprio 0\n"
      "buffer_load_dwordx4 v[184:187], v5, s[60:63], s97 offen sc1\n"
      "s_setprio 1\n"
      "s_waitcnt lgkmcnt(7)\n"
      "v_mfma_f32_32x32x16_bf16 v[64:79], v[96:99], v[224:227], v[64:79]\n"
      "s_waitcnt lgkmcnt(6)\n"
      "v_mfma_f32_32x32x16_bf16 v[80:95], v[100:103], v[224:227], v[80:95]\n"
      "ds_read_b128 v[96:99], v5 offset:24576\n"
      "ds_read_b128 v[100:103], v5 offset:25600\n"
      "s_add_u32 s97, s79, 122880\n"
      "s_setprio 0\n"
      "buffer_load_dwordx4 v[32:35], v5, s[60:63], s97 offen sc1\n"
      "s_setprio 1\n"
      "s_waitcnt lgkmcnt(7)\n"
      "v_mfma_f32_32x32x16_bf16 v[64:79], v[112:115], v[228:231], v[64:79]\n"
      "s_waitcnt lgkmcnt(6)\n"
      "v_mfma_f32_32x32x16_bf16 v[80:95], v[116:119], v[228:231], v[80:95]\n"
      "ds_read_b128 v[112:115], v5 offset:26624\n"
      "ds_read_b128 v[116:119], v5 offset:27648\n"
      "s_add_u32 s97, s79, 122880\n"
      "s_setprio 0\n"
      "buffer_load_dwordx4 v[36:39], v5, s[60:63], s97 offen offset:1024 sc1\n"
      "s_setprio 1\n"
      "s_waitcnt lgkmcnt(7)\n"
      "v_mfma_f32_32x32x16_bf16 v[64:79], v[104:107], v[232:235], v[64:79]\n"
      "s_waitcnt lgkmcnt(6)\n"
      "v_mfma_f32_32x32x16_bf16 v[80:95], v[108:111], v[232:235], v[80:95]\n"
      "ds_read_b128 v[104:107], v5 offset:28672\n"
      "ds_read_b128 v[108:111], v5 offset:29696\n"
      "s_add_u32 s97, s79, 122880\n"
      "s_setprio 0\n"
      "buffer_load_dwordx4 v[40:43], v5, s[60:63], s97 offen offset:2048 sc1\n"
      "s_setprio 1\n"
      "s_waitcnt lgkmcnt(7)\n"
      "v_mfma_f32_32x32x16_bf16 v[64:79], v[120:123], v[236:239], v[64:79]\n"
      "s_waitcnt lgkmcnt(6)\n"
      "v_mfma_f32_32x32x16_bf16 v[80:95], v[124:127], v[236:239], v[80:95]\n"
      "ds_read_b128 v[120:123], v5 offset:30720\n"
      "ds_read_b128 v[124:127], v5 offset:31744\n"
      "s_add_u32 s97, s79, 122880\n"
      "s_setprio 0\n"
      "buffer_load_dwordx4 v[44:47], v5, s[60:63], s97 offen offset:3072 sc1\n"
      "s_setprio 1\n"
      "s_add_u32 s97, s79, 126976\n"
      "s_setprio 0\n"
      "buffer_load_dwordx4 v[48:51], v5, s[60:63], s97 offen sc1\n"
      "s_setprio 1\n"
      "s_cmp_lg_u32 s64, 3\n"
      "s_cbranch_scc1 Lai_9\n"
      "s_lshl_b32 s33, s65, 2\n"
      "s_add_u32 s33, s33, s70\n"
      "s_lshl_b32 s33, s33, 8\n"
      "s_add_u32 s33, s33, 12288\n"
      "v_min_u32 v12, 53, v0\n"
      "v_lshlrev_b32 v12, 2, v12\n"
      "buffer_load_dword v13, v12, s[60:63], s33 offen sc1\n"
      "Lai_9:\n"
      "s_waitcnt lgkmcnt(7)\n"
      "v_mfma_f32_32x32x16_bf16 v[64:79], v[96:99], v[240:243], v[64:79]\n"
      "s_waitcnt lgkmcnt(6)\n"
      "v_mfma_f32_32x32x16_bf16 v[80:95], v[100:103], v[240:243], v[80:95]\n"
      "s_waitcnt lgkmcnt(5)\n"
      "v_mfma_f32_32x32x16_bf16 v[64:79], v[112:115], v[244:247], v[64:79]\n"
      "s_waitcnt lgkmcnt(4)\n"
      "v_mfma_f32_32x32x16_bf16 v[80:95], v[116:119], v[244:247], v[80:95]\n"
      "s_waitcnt lgkmcnt(3)\n"
      "v_mfma_f32_32x32x16_bf16 v[64:79], v[104:107], v[248:251], v[64:79]\n"
      "s_waitcnt lgkmcnt(2)\n"
      "v_mfma_f32_32x32x16_bf16 v[80:95], v[108:111], v[248:251], v[80:95]\n"
      "s_waitcnt lgkmcnt(1)\n"
      "v_mfma_f32_32x32x16_bf16 v[64:79], v[120:123], v[252:255], v[64:79]\n"
      "s_waitcnt lgkmcnt(0)\n"
      "v_mfma_f32_32x32x16_bf16 v[80:95], v[124:127], v[252:255], v[80:95]\n"
      "L_afterg0:\n"
      "s_cmp_lt_u32 s64, 4\n"
      "s_cbranch_scc1 L_fb1\n"
      "s_barrier\n"
      "s_setprio 2\n"
      "L_fb1:\n"
      "s_mov_b32 s8, 0x3fb8aa3b\n"
      "s_mov_b32 s9, s8\n"
      "s_nop 5\n"
      "v_max3_f32 v6, v64, v65, v66\n"
      "v_min3_f32 v8, v64, v65, v66\n"
      "v_max3_f32 v6, v6, v67, v68\n"
      "v_min3_f32 v8, v8, v67, v68\n"
      "v_max3_f32 v6, v6, v69, v70\n"
      "v_min3_f32 v8, v8, v69, v70\n"
      "v_max3_f32 v6, v6, v71, v72\n"
      "v_min3_f32 v8, v8, v71, v72\n"
      "v_max3_f32 v6, v6, v73, v74\n"
      "v_min3_f32 v8, v8, v73, v74\n"
      "v_max3_f32 v6, v6, v75, v76\n"
      "v_min3_f32 v8, v8, v75, v76\n"
      "v_max3_f32 v6, v6, v77, v78\n"
      "v_min3_f32 v8, v8, v77, v78\n"
      "v_max3_f32 v6, v6, v79, v80\n"
      "v_min3_f32 v8, v8, v79, v80\n"
      "v_max3_f32 v6, v6, v81, v82\n"
      "v_min3_f32 v8, v8, v81, v82\n"
      "v_max_f32 v6, v6, v83\n"
      "v_min_f32 v8, v8, v83\n"
      "v_mul_f32 v6, s8, v6\n"
      "v_mul_f32 v8, s8, v8\n"
      "v_mul_f32 v28, s8, v94\n"
      "v_cvt_pk_bf16_f32 v212, v84, v85\n"
      "v_cvt_pk_bf16_f32 v213, v86, v87\n"
      "v_cvt_pk_bf16_f32 v214, v88, v89\n"
      "v_cvt_pk_bf16_f32 v215, v90, v91\n"
      "v_cvt_pk_bf16_f32 v216, v92, v93\n"
      "v_mov_b32 v217, 0x3f800000\n"
      "v_fma_f32 v192, v64, s8, -v6\n"
      "v_fma_f32 v193, v65, s8, -v6\n"
      "v_fma_f32 v194, v66, s8, -v6\n"
      "v_fma_f32 v195, v67, s8, -v6\n"
      "v_fma_f32 v196, v68, s8, -v6\n"
      "v_fma_f32 v197, v69, s8, -v6\n"
      "v_fma_f32 v198, v70, s8, -v6\n"
      "v_fma_f32 v199, v71, s8, -v6\n"
      "v_fma_f32 v200, v72, s8, -v6\n"
      "v_fma_f32 v201, v73, s8, -v6\n"
      "v_fma_f32 v202, v74, s8, -v6\n"
      "v_fma_f32 v203, v75, s8, -v6\n"
      "v_fma_f32 v204, v76, s8, -v6\n"
      "v_fma_f32 v205, v77, s8, -v6\n"
      "v_fma_f32 v206, v78, s8, -v6\n"
      "v_fma_f32 v207, v79, s8, -v6\n"
      "v_fma_f32 v208, v80, s8, -v6\n"
      "v_fma_f32 v209, v81, s8, -v6\n"
      "v_fma_f32 v210, v82, s8, -v6\n"
      "v_fma_f32 v211, v83, s8, -v6\n"
      "v_fma_f32 v64, -v64, s8, v8\n"
      "v_fma_f32 v65, -v65, s8, v8\n"
      "v_fma_f32 v66, -v66, s8, v8\n"
      "v_fma_f32 v67, -v67, s8, v8\n"
      "v_fma_f32 v68, -v68, s8, v8\n"
      "v_fma_f32 v69, -v69, s8, v8\n"
      "v_fma_f32 v70, -v70, s8, v8\n"
      "v_fma_f32 v71, -v71, s8, v8\n"
      "v_fma_f32 v72, -v72, s8, v8\n"
      "v_fma_f32 v73, -v73, s8, v8\n"
      "v_fma_f32 v74, -v74, s8, v8\n"
      "v_fma_f32 v75, -v75, s8, v8\n"
      "v_fma_f32 v76, -v76, s8, v8\n"
      "v_fma_f32 v77, -v77, s8, v8\n"
      "v_fma_f32 v78, -v78, s8, v8\n"
      "v_fma_f32 v79, -v79, s8, v8\n"
      "v_fma_f32 v80, -v80, s8, v8\n"
      "v_fma_f32 v81, -v81, s8, v8\n"
      "v_fma_f32 v82, -v82, s8, v8\n"
      "v_fma_f32 v83, -v83, s8, v8\n"
      "v_exp_f32 v28, -v28\n"
      "v_exp_f32 v64, v64\n"
      "v_exp_f32 v65, v65\n"
      "v_exp_f32 v66, v66\n"
      "v_exp_f32 v67, v67\n"
      "v_exp_f32 v68, v68\n"
      "v_exp_f32 v69, v69\n"
      "v_exp_f32 v70, v70\n"
      "v_exp_f32 v71, v71\n"
      "v_exp_f32 v72, v72\n"
      "v_exp_f32 v73, v73\n"
      "v_exp_f32 v74, v74\n"
      "v_exp_f32 v75, v75\n"
      "v_exp_f32 v76, v76\n"
      "v_exp_f32 v77, v77\n"
      "v_exp_f32 v78, v78\n"
      "v_exp_f32 v79, v79\n"
      "v_exp_f32 v80, v80\n"
      "v_exp_f32 v81, v81\n"
      "v_exp_f32 v82, v82\n"
      "v_exp_f32 v83, v83\n"
      "v_exp_f32 v192, v192\n"
      "v_exp_f32 v193, v193\n"
      "v_exp_f32 v194, v194\n"
      "v_exp_f32 v195, v195\n"
      "v_exp_f32 v196, v196\n"
      "v_exp_f32 v197, v197\n"
      "v_exp_f32 v198, v198\n"
      "v_exp_f32 v199, v199\n"
      "v_exp_f32 v200, v200\n"
      "v_exp_f32 v201, v201\n"
      "v_exp_f32 v202, v202\n"
      "v_exp_f32 v203, v203\n"
      "v_exp_f32 v204, v204\n"
      "v_exp_f32 v205, v205\n"
      "v_exp_f32 v206, v206\n"
      "v_exp_f32 v207, v207\n"
      "v_exp_f32 v208, v208\n"
      "v_exp_f32 v209, v209\n"
      "v_exp_f32 v210, v210\n"
      "v_exp_f32 v211, v211\n"
      "s_cmp_ge_u32 s64, 4\n"
      "s_cbranch_scc1 L_fa1\n"
      "s_cmp_lg_u32 s64, 3\n"
      "s_cbranch_scc1 Lacd_12\n"
      "s_waitcnt vmcnt(0)\n"
      "s_mov_b32 s96, 20000\n"
      "Lac_11:\n"
      "v_cmp_ne_u32 vcc, s26, v13\n"
      "s_cmp_eq_u64 vcc, 0\n"
      "s_cbranch_scc1 Lacd_12\n"
      "s_sub_u32 s96, s96, 1\n"
      "s_cmp_eq_u32 s96, 0\n"
      "s_cbranch_scc1 Lacd_12\n"
      "s_sleep 4\n"
      "buffer_load_dword v13, v12, s[60:63], s33 offen sc1\n"
      "s_waitcnt vmcnt(0)\n"
      "s_branch Lac_11\n"
      "Lacd_12:\n"
      "s_barrier\n"
      "s_add_u32 s97, s78, 0\n"
      "s_setprio 0\n"
      "buffer_load_dwordx4 v[96:99], v5, s[60:63], s97 offen sc1\n"
      "s_setprio 1\n"
      "s_setprio 0\n"
      "buffer_load_dwordx4 v[100:103], v5, s[60:63], s97 offen offset:1024 sc1\n"
      "s_setprio 1\n"
      "s_setprio 0\n"
      "buffer_load_dwordx4 v[104:107], v5, s[60:63], s97 offen offset:2048 sc1\n"
      "s_setprio 1\n"
      "s_setprio 0\n"
      "buffer_load_dwordx4 v[108:111], v5, s[60:63], s97 offen offset:3072 sc1\n"
      "s_setprio 1\n"
      "s_add_u32 s97, s78, 32768\n"
      "s_setprio 0\n"
      "buffer_load_dwordx4 v[112:115], v5, s[60:63], s97 offen sc1\n"
      "s_setprio 1\n"
      "s_setprio 0\n"
      "buffer_load_dwordx4 v[116:119], v5, s[60:63], s97 offen offset:1024 sc1\n"
      "s_setprio 1\n"
      "s_setprio 0\n"
      "buffer_load_dwordx4 v[120:123], v5, s[60:63], s97 offen offset:2048 sc1\n"
      "s_setprio 1\n"
      "s_setprio 0\n"
      "buffer_load_dwordx4 v[124:127], v5, s[60:63], s97 offen offset:3072 sc1\n"
      "s_setprio 1\n"
      "s_mov_b32 m0, s10\n"
      "s_add_u32 s97, s78, 65536\n"
      "s_setprio 0\n"
      "buffer_load_dwordx4 v5, s[60:63], s97 offen sc1 lds\n"
      "s_setprio 1\n"
      "s_setprio 0\n"
      "buffer_load_dwordx4 v5, s[60:63], s97 offen offset:1024 sc1 lds\n"
      "s_setprio 1\n"
      "s_setprio 0\n"
      "buffer_load_dwordx4 v5, s[60:63], s97 offen offset:2048 sc1 lds\n"
      "s_setprio 1\n"
      "s_add_u32 m0, s27, 7168\n"
      "s_add_u32 s97, s78, 65536\n"
      "s_setprio 0\n"
      "buffer_load_dwordx4 v5, s[60:63], s97 offen offset:3072 sc1 lds\n"
      "s_setprio 1\n"
      "L_fa1:\n"
      "v_add_f32 v28, 1.0, v28\n"
      "v_pk_add_f32 v[220:221], v[64:65], v[66:67]\n"
      "v_pk_add_f32 v[218:219], v[192:193], v[194:195]\n"
      "v_rcp_f32 v28, v28\n"
      "v_pk_add_f32 v[220:221], v[220:221], v[68:69]\n"
      "v_pk_add_f32 v[218:219], v[218:219], v[196:197]\n"
      "v_pk_add_f32 v[220:221], v[220:221], v[70:71]\n"
      "v_pk_add_f32 v[218:219], v[218:219], v[198:199]\n"
      "v_pk_add_f32 v[220:221], v[220:221], v[72:73]\n"
      "v_pk_add_f32 v[218:219], v[218:219], v[200:201]\n"
      "v_pk_add_f32 v[220:221], v[220:221], v[74:75]\n"
      "v_pk_add_f32 v[218:219], v[218:219], v[202:203]\n"
      "v_pk_add_f32 v[220:221], v[220:221], v[76:77]\n"
      "v_pk_add_f32 v[218:219], v[218:219], v[204:205]\n"
      "v_pk_add_f32 v[220:221], v[220:221], v[78:79]\n"
      "v_pk_add_f32 v[218:219], v[218:219], v[206:207]\n"
      "v_pk_add_f32 v[220:221], v[220:221], v[80:81]\n"
      "v_pk_add_f32 v[218:219], v[218:219], v[208:209]\n"
      "v_pk_add_f32 v[220:221], v[220:221], v[82:83]\n"
      "v_pk_add_f32 v[218:219], v[218:219], v[210:211]\n"
      "v_add_f32 v8, v220, v221\n"
      "v_add_f32 v6, v218, v219\n"
      "v_rcp_f32 v8, v8\n"
      "v_rcp_f32 v6, v6\n"
      "v_sub_f32 v31, 1.0, v28\n"
      "ds_write_b32 v27, v31\n"
      "ds_write_b64 v17, v[212:213] offset:80\n"
      "ds_write_b64 v17, v[214:215] offset:88\n"
      "ds_write_b64 v17, v[216:217] offset:96\n"
      "v_lshlrev_b32 v218, 2, v0\n"
      "v_mov_b32 v219, 0\n"
      "s_mov_b64 exec, 0xfffff\n"
      "ds_write_b32 v218, v219 offset:38624\n"
      "s_mov_b64 exec, -1\n"
      "v_pk_mul_f32 v[64:65], v[64:65], v[8:9] op_sel_hi:[1,0]\n"
      "v_pk_mul_f32 v[66:67], v[66:67], v[8:9] op_sel_hi:[1,0]\n"
      "v_pk_mul_f32 v[68:69], v[68:69], v[8:9] op_sel_hi:[1,0]\n"
      "v_pk_mul_f32 v[70:71], v[70:71], v[8:9] op_sel_hi:[1,0]\n"
      "v_cvt_pk_bf16_f32 v64, v64, v65\n"
      "v_cvt_pk_bf16_f32 v65, v66, v67\n"
      "v_cvt_pk_bf16_f32 v66, v68, v69\n"
      "v_cvt_pk_bf16_f32 v67, v70, v71\n"
      "s_mov_b32 exec_lo, 0\n"
      "ds_write_b128 v16, v[64:67] offset:0\n"
      "s_mov_b32 exec_lo, -1\n"
      "v_pk_mul_f32 v[72:73], v[72:73], v[8:9] op_sel_hi:[1,0]\n"
      "v_pk_mul_f32 v[74:75], v[74:75], v[8:9] op_sel_hi:[1,0]\n"
      "v_pk_mul_f32 v[76:77], v[76:77], v[8:9] op_sel_hi:[1,0]\n"
      "v_pk_mul_f32 v[78:79], v[78:79], v[8:9] op_sel_hi:[1,0]\n"
      "v_cvt_pk_bf16_f32 v68, v72, v73\n"
      "v_cvt_pk_bf16_f32 v69, v74, v75\n"
      "v_cvt_pk_bf16_f32 v70, v76, v77\n"
      "v_cvt_pk_bf16_f32 v71, v78, v79\n"
      "s_mov_b32 exec_lo, 0\n"
      "ds_write_b128 v16, v[68:71] offset:16\n"
      "s_mov_b32 exec_lo, -1\n"
      "v_pk_mul_f32 v[80:81], v[80:81], v[8:9] op_sel_hi:[1,0]\n"
      "v_pk_mul_f32 v[82:83], v[82:83], v[8:9] op_sel_hi:[1,0]\n"
      "v_pk_mul_f32 v[192:193], v[192:193], v[6:7] op_sel_hi:[1,0]\n"
      "v_pk_mul_f32 v[194:195], v[194:195], v[6:7] op_sel_hi:[1,0]\n"
      "v_cvt_pk_bf16_f32 v72, v80, v81\n"
      "v_cvt_pk_bf16_f32 v73, v82, v83\n"
      "v_cvt_pk_bf16_f32 v74, v192, v193\n"
      "v_cvt_pk_bf16_f32 v75, v194, v195\n"
      "s_mov_b32 exec_lo, 0\n"
      "ds_write_b128 v16, v[72:75] offset:32\n"
      "s_mov_b32 exec_lo, -1\n"
      "v_pk_mul_f32 v[196:197], v[196:197], v[6:7] op_sel_hi:[1,0]\n"
      "v_pk_mul_f32 v[198:199], v[198:199], v[6:7] op_sel_hi:[1,0]\n"
      "v_pk_mul_f32 v[200:201], v[200:201], v[6:7] op_sel_hi:[1,0]\n"
      "v_pk_mul_f32 v[202:203], v[202:203], v[6:7] op_sel_hi:[1,0]\n"
      "v_cvt_pk_bf16_f32 v76, v196, v197\n"
      "v_cvt_pk_bf16_f32 v77, v198, v199\n"
      "v_cvt_pk_bf16_f32 v78, v200, v201\n"
      "v_cvt_pk_bf16_f32 v79, v202, v203\n"
      "s_mov_b32 exec_lo, 0\n"
      "ds_write_b128 v16, v[76:79] offset:48\n"
      "s_mov_b32 exec_lo, -1\n"
      "v_pk_mul_f32 v[204:205], v[204:205], v[6:7] op_sel_hi:[1,0]\n"
      "v_pk_mul_f32 v[206:207], v[206:207], v[6:7] op_sel_hi:[1,0]\n"
      "v_pk_mul_f32 v[208:209], v[208:209], v[6:7] op_sel_hi:[1,0]\n"
      "v_pk_mul_f32 v[210:211], v[210:211], v[6:7] op_sel_hi:[1,0]\n"
      "v_cvt_pk_bf16_f32 v80, v204, v205\n"
      "v_cvt_pk_bf16_f32 v81, v206, v207\n"
      "v_cvt_pk_bf16_f32 v82, v208, v209\n"
      "v_cvt_pk_bf16_f32 v83, v210, v211\n"
      "s_mov_b32 exec_lo, 0\n"
      "ds_write_b128 v16, v[80:83] offset:64\n"
      "s_mov_b32 exec_lo, -1\n"
      "s_cmp_lt_u32 s64, 4\n"
      "s_cbranch_scc1 L_fa2\n"
      "s_add_u32 s97, s78, 0\n"
      "s_setprio 0\n"
      "buffer_load_dwordx4 v[96:99], v5, s[60:63], s97 offen sc1\n"
      "s_setprio 1\n"
      "s_setprio 0\n"
      "buffer_load_dwordx4 v[100:103], v5, s[60:63], s97 offen offset:1024 sc1\n"
      "s_setprio 1\n"
      "s_setprio 0\n"
      "buffer_load_dwordx4 v[104:107], v5, s[60:63], s97 offen offset:2048 sc1\n"
      "s_setprio 1\n"
      "s_setprio 0\n"
      "buffer_load_dwordx4 v[108:111], v5, s[60:63], s97 offen offset:3072 sc1\n"
      "s_setprio 1\n"
      "s_add_u32 s97, s78, 32768\n"
      "s_setprio 0\n"
      "buffer_load_dwordx4 v[112:115], v5, s[60:63], s97 offen sc1\n"
      "s_setprio 1\n"
      "s_setprio 0\n"
      "buffer_load_dwordx4 v[116:119], v5, s[60:63], s97 offen offset:1024 sc1\n"
      "s_setprio 1\n"
      "s_setprio 0\n"
      "buffer_load_dwordx4 v[120:123], v5, s[60:63], s97 offen offset:2048 sc1\n"
      "s_setprio 1\n"
      "s_setprio 0\n"
      "buffer_load_dwordx4 v[124:127], v5, s[60:63], s97 offen offset:3072 sc1\n"
      "s_setprio 1\n"
      "s_mov_b32 m0, s10\n"
      "s_add_u32 s97, s78, 65536\n"
      "s_setprio 0\n"
      "buffer_load_dwordx4 v5, s[60:63], s97 offen sc1 lds\n"
      "s_setprio 1\n"
      "s_setprio 0\n"
      "buffer_load_dwordx4 v5, s[60:63], s97 offen offset:1024 sc1 lds\n"
      "s_setprio 1\n"
      "s_setprio 0\n"
      "buffer_load_dwordx4 v5, s[60:63], s97 offen offset:2048 sc1 lds\n"
      "s_setprio 1\n"
      "s_add_u32 m0, s27, 7168\n"
      "s_add_u32 s97, s78, 65536\n"
      "s_setprio 0\n"
      "buffer_load_dwordx4 v5, s[60:63], s97 offen offset:3072 sc1 lds\n"
      "s_setprio 1\n"
      "L_fa2:\n"
      "v_add_u32 v27, s10, v5\n"
      "s_waitcnt lgkmcnt(0)\n"
      "s_barrier\n"
      "s_cmp_lt_u32 s64, 3\n"
      "s_cselect_b32 s5, 1, 0\n"
      "s_cbranch_scc0 L_nokv\n"
      "s_setprio 2\n"
      "s_lshl_b32 s73, s64, 5\n"
      "v_add_u32 v6, s73, v18\n"
      "ds_read_b64_tr_b16 v[192:193], v6 offset:0\n"
      "ds_read_b64_tr_b16 v[194:195], v6 offset:576\n"
      "ds_read_b64_tr_b16 v[196:197], v18 offset:80\n"
      "ds_read_b64_tr_b16 v[198:199], v18 offset:656\n"
      "ds_read_b64_tr_b16 v[200:201], v18 offset:112\n"
      "ds_read_b64_tr_b16 v[202:203], v18 offset:688\n"
      "ds_read_b64_tr_b16 v[204:205], v6 offset:4608\n"
      "ds_read_b64_tr_b16 v[206:207], v6 offset:5184\n"
      "ds_read_b64_tr_b16 v[208:209], v18 offset:4688\n"
      "ds_read_b64_tr_b16 v[210:211], v18 offset:5264\n"
      "ds_read_b64_tr_b16 v[212:213], v18 offset:4720\n"
      "ds_read_b64_tr_b16 v[214:215], v18 offset:5296\n"
      "v_mov_b32 v251, s26\n"
      "v_mov_b32 v255, s26\n"
      "s_mov_b32 s74, 0x7060501\n"
      "s_mov_b32 s75, 0x7060502\n"
      "s_mov_b32 s76, 0x7060503\n"
      "ds_read_b64_tr_b16 v[216:217], v6 offset:9216\n"
      "ds_read_b64_tr_b16 v[218:219], v6 offset:9792\n"
      "ds_read_b64_tr_b16 v[220:221], v18 offset:9296\n"
      "ds_read_b64_tr_b16 v[222:223], v18 offset:9872\n"
      "ds_read_b64_tr_b16 v[224:225], v18 offset:9328\n"
      "ds_read_b64_tr_b16 v[226:227], v18 offset:9904\n"
      "s_waitcnt lgkmcnt(12)\n"
      "v_mfma_f32_16x16x32_bf16 v[240:243], v[192:195], v[196:199], 0\n"
      "v_mfma_f32_16x16x32_bf16 v[244:247], v[192:195], v[200:203], 0\n"
      "ds_read_b64_tr_b16 v[228:229], v6 offset:13824\n"
      "ds_read_b64_tr_b16 v[230:231], v6 offset:14400\n"
      "ds_read_b64_tr_b16 v[232:233], v18 offset:13904\n"
      "ds_read_b64_tr_b16 v[234:235], v18 offset:14480\n"
      "ds_read_b64_tr_b16 v[236:237], v18 offset:13936\n"
      "ds_read_b64_tr_b16 v[238:239], v18 offset:14512\n"
      "s_waitcnt lgkmcnt(12)\n"
      "v_mfma_f32_16x16x32_bf16 v[240:243], v[204:207], v[208:211], v[240:243]\n"
      "v_mfma_f32_16x16x32_bf16 v[244:247], v[204:207], v[212:215], v[244:247]\n"
      "ds_read_b64_tr_b16 v[192:193], v6 offset:18432\n"
      "ds_read_b64_tr_b16 v[194:195], v6 offset:19008\n"
      "ds_read_b64_tr_b16 v[196:197], v18 offset:18512\n"
      "ds_read_b64_tr_b16 v[198:199], v18 offset:19088\n"
      "ds_read_b64_tr_b16 v[200:201], v18 offset:18544\n"
      "ds_read_b64_tr_b16 v[202:203], v18 offset:19120\n"
      "s_waitcnt lgkmcnt(12)\n"
      "v_mfma_f32_16x16x32_bf16 v[240:243], v[216:219], v[220:223], v[240:243]\n"
      "v_mfma_f32_16x16x32_bf16 v[244:247], v[216:219], v[224:227], v[244:247]\n"
      "ds_read_b64_tr_b16 v[204:205], v6 offset:23040\n"
      "ds_read_b64_tr_b16 v[206:207], v6 offset:23616\n"
      "ds_read_b64_tr_b16 v[208:209], v18 offset:23120\n"
      "ds_read_b64_tr_b16 v[210:211], v18 offset:23696\n"
      "ds_read_b64_tr_b16 v[212:213], v18 offset:23152\n"
      "ds_read_b64_tr_b16 v[214:215], v18 offset:23728\n"
      "s_waitcnt lgkmcnt(12)\n"
      "v_mfma_f32_16x16x32_bf16 v[240:243], v[228:231], v[232:235], v[240:243]\n"
      "v_mfma_f32_16x16x32_bf16 v[244:247], v[228:231], v[236:239], v[244:247]\n"
      "ds_read_b64_tr_b16 v[216:217], v6 offset:27648\n"
      "ds_read_b64_tr_b16 v[218:219], v6 offset:28224\n"
      "ds_read_b64_tr_b16 v[220:221], v18 offset:27728\n"
      "ds_read_b64_tr_b16 v[222:223], v18 offset:28304\n"
      "ds_read_b64_tr_b16 v[224:225], v18 offset:27760\n"
      "ds_read_b64_tr_b16 v[226:227], v18 offset:28336\n"
      "s_waitcnt lgkmcnt(12)\n"
      "v_mfma_f32_16x16x32_bf16 v[240:243], v[192:195], v[196:199], v[240:243]\n"
      "v_mfma_f32_16x16x32_bf16 v[244:247], v[192:195], v[200:203], v[244:247]\n"
      "ds_read_b64_tr_b16 v[228:229], v6 offset:32256\n"
      "ds_read_b64_tr_b16 v[230:231], v6 offset:32832\n"
      "ds_read_b64_tr_b16 v[232:233], v18 offset:32336\n"
      "ds_read_b64_tr_b16 v[234:235], v18 offset:32912\n"
      "ds_read_b64_tr_b16 v[236:237], v18 offset:32368\n"
      "ds_read_b64_tr_b16 v[238:239], v18 offset:32944\n"
      "s_waitcnt lgkmcnt(12)\n"
      "v_mfma_f32_16x16x32_bf16 v[240:243], v[204:207], v[208:211], v[240:243]\n"
      "v_mfma_f32_16x16x32_bf16 v[244:247], v[204:207], v[212:215], v[244:247]\n"
      "s_waitcnt lgkmcnt(6)\n"
      "v_mfma_f32_16x16x32_bf16 v[240:243], v[216:219], v[220:223], v[240:243]\n"
      "v_mfma_f32_16x16x32_bf16 v[244:247], v[216:219], v[224:227], v[244:247]\n"
      "s_waitcnt lgkmcnt(0)\n"
      "v_mfma_f32_16x16x32_bf16 v[240:243], v[228:231], v[232:235], v[240:243]\n"
      "v_mfma_f32_16x16x32_bf16 v[244:247], v[228:231], v[236:239], v[244:247]\n"
      "s_nop 6\n"
      "v_perm_b32 v248, v240, v243, s74\n"
      "v_perm_b32 v249, v241, v243, s75\n"
      "v_perm_b32 v250, v242, v243, s76\n"
      "v_perm_b32 v252, v244, v247, s74\n"
      "v_perm_b32 v253, v245, v247, s75\n"
      "v_perm_b32 v254, v246, v247, s76\n"
      "s_mov_b64 exec, s[16:17]\n"
      "buffer_store_dwordx4 v[248:251], v19, s[60:63], s22 offen sc1\n"
      "s_mov_b64 exec, s[18:19]\n"
      "buffer_store_dwordx4 v[252:255], v19, s[60:63], s22 offen offset:256 sc1\n"
      "s_mov_b64 exec, -1\n"
      "s_setprio 1\n"
      "L_nokv:\n"
      "ds_read_b32 v8, v30 offset:0\n"
      "ds_read_b32 v10, v30 offset:64\n"
      "ds_read_b32 v12, v30 offset:128\n"
      "ds_read_b32 v14, v30 offset:192\n"
      "s_cmp_lg_u32 s5, 0\n"
      "s_cbranch_scc1 Lxw_13\n"
      "s_waitcnt vmcnt(28)\n"
      "s_branch Lxj_14\n"
      "Lxw_13:\n"
      "s_waitcnt vmcnt(30)\n"
      "Lxj_14:\n"
      "s_cmp_lg_u32 s5, 0\n"
      "s_cbranch_scc1 Lxw_15\n"
      "s_waitcnt vmcnt(8)\n"
      "s_branch Lxj_16\n"
      "Lxw_15:\n"
      "s_waitcnt vmcnt(10)\n"
      "Lxj_16:\n"
      "v_mfma_f32_16x16x32_bf16 v[188:191], v[128:131], v[96:99], 0\n"
      "v_mfma_f32_16x16x32_bf16 v[208:211], v[128:131], v[100:103], 0\n"
      "s_add_u32 m0, s27, 5120\n"
      "s_add_u32 s97, s79, 204800\n"
      "s_setprio 0\n"
      "buffer_load_dwordx4 v5, s[60:63], s97 offen sc1 lds\n"
      "s_setprio 1\n"
      "v_mfma_f32_16x16x32_bf16 v[228:231], v[128:131], v[104:107], 0\n"
      "v_mfma_f32_16x16x32_bf16 v[248:251], v[128:131], v[108:111], 0\n"
      "s_add_u32 m0, s27, 5120\n"
      "s_add_u32 s97, s79, 204800\n"
      "s_setprio 0\n"
      "buffer_load_dwordx4 v5, s[60:63], s97 offen offset:1024 sc1 lds\n"
      "s_setprio 1\n"
      "v_mfma_f32_16x16x32_bf16 v[192:195], v[132:135], v[96:99], 0\n"
      "v_mfma_f32_16x16x32_bf16 v[212:215], v[132:135], v[100:103], 0\n"
      "s_add_u32 m0, s27, 5120\n"
      "s_add_u32 s97, s79, 204800\n"
      "s_setprio 0\n"
      "buffer_load_dwordx4 v5, s[60:63], s97 offen offset:2048 sc1 lds\n"
      "s_setprio 1\n"
      "v_mfma_f32_16x16x32_bf16 v[232:235], v[132:135], v[104:107], 0\n"
      "v_mfma_f32_16x16x32_bf16 v[252:255], v[132:135], v[108:111], 0\n"
      "s_add_u32 m0, s27, 5120\n"
      "s_add_u32 s97, s79, 204800\n"
      "s_setprio 0\n"
      "buffer_load_dwordx4 v5, s[60:63], s97 offen offset:3072 sc1 lds\n"
      "s_setprio 1\n"
      "v_mfma_f32_16x16x32_bf16 v[196:199], v[136:139], v[96:99], 0\n"
      "v_mfma_f32_16x16x32_bf16 v[216:219], v[136:139], v[100:103], 0\n"
      "s_add_u32 m0, s27, 9216\n"
      "s_add_u32 s97, s79, 208896\n"
      "s_setprio 0\n"
      "buffer_load_dwordx4 v5, s[60:63], s97 offen sc1 lds\n"
      "s_setprio 1\n"
      "v_mfma_f32_16x16x32_bf16 v[236:239], v[136:139], v[104:107], 0\n"
      "v_mfma_f32_16x16x32_bf16 v[84:87], v[136:139], v[108:111], 0\n"
      "v_mfma_f32_16x16x32_bf16 v[200:203], v[140:143], v[96:99], 0\n"
      "v_mfma_f32_16x16x32_bf16 v[220:223], v[140:143], v[100:103], 0\n"
      "v_mfma_f32_16x16x32_bf16 v[240:243], v[140:143], v[104:107], 0\n"
      "v_mfma_f32_16x16x32_bf16 v[88:91], v[140:143], v[108:111], 0\n"
      "v_mfma_f32_16x16x32_bf16 v[204:207], v[144:147], v[96:99], 0\n"
      "v_mfma_f32_16x16x32_bf16 v[224:227], v[144:147], v[100:103], 0\n"
      "v_mfma_f32_16x16x32_bf16 v[244:247], v[144:147], v[104:107], 0\n"
      "v_mfma_f32_16x16x32_bf16 v[92:95], v[144:147], v[108:111], 0\n"
      "s_cmp_lg_u32 s5, 0\n"
      "s_cbranch_scc1 Lxw_17\n"
      "s_waitcnt vmcnt(5)\n"
      "s_branch Lxj_18\n"
      "Lxw_17:\n"
      "s_waitcnt vmcnt(7)\n"
      "Lxj_18:\n"
      "ds_read_b128 v[96:99], v27 offset:0\n"
      "ds_read_b128 v[100:103], v27 offset:1024\n"
      "ds_read_b128 v[104:107], v27 offset:2048\n"
      "ds_read_b128 v[108:111], v20 offset:10240\n"
      "v_mfma_f32_16x16x32_bf16 v[188:191], v[148:151], v[112:115], v[188:191]\n"
      "v_mfma_f32_16x16x32_bf16 v[208:211], v[148:151], v[116:119], v[208:211]\n"
      "v_mfma_f32_16x16x32_bf16 v[228:231], v[148:151], v[120:123], v[228:231]\n"
      "v_mfma_f32_16x16x32_bf16 v[248:251], v[148:151], v[124:127], v[248:251]\n"
      "v_mfma_f32_16x16x32_bf16 v[192:195], v[152:155], v[112:115], v[192:195]\n"
      "v_mfma_f32_16x16x32_bf16 v[212:215], v[152:155], v[116:119], v[212:215]\n"
      "v_mfma_f32_16x16x32_bf16 v[232:235], v[152:155], v[120:123], v[232:235]\n"
      "v_mfma_f32_16x16x32_bf16 v[252:255], v[152:155], v[124:127], v[252:255]\n"
      "v_mfma_f32_16x16x32_bf16 v[196:199], v[156:159], v[112:115], v[196:199]\n"
      "v_mfma_f32_16x16x32_bf16 v[216:219], v[156:159], v[116:119], v[216:219]\n"
      "v_mfma_f32_16x16x32_bf16 v[236:239], v[156:159], v[120:123], v[236:239]\n"
      "v_mfma_f32_16x16x32_bf16 v[84:87], v[156:159], v[124:127], v[84:87]\n"
      "v_mfma_f32_16x16x32_bf16 v[200:203], v[160:163], v[112:115], v[200:203]\n"
      "v_mfma_f32_16x16x32_bf16 v[220:223], v[160:163], v[116:119], v[220:223]\n"
      "v_mfma_f32_16x16x32_bf16 v[240:243], v[160:163], v[120:123], v[240:243]\n"
      "v_mfma_f32_16x16x32_bf16 v[88:91], v[160:163], v[124:127], v[88:91]\n"
      "v_mfma_f32_16x16x32_bf16 v[204:207], v[164:167], v[112:115], v[204:207]\n"
      "v_mfma_f32_16x16x32_bf16 v[224:227], v[164:167], v[116:119], v[224:227]\n"
      "v_mfma_f32_16x16x32_bf16 v[244:247], v[164:167], v[120:123], v[244:247]\n"
      "v_mfma_f32_16x16x32_bf16 v[92:95], v[164:167], v[124:127], v[92:95]\n"
      "s_waitcnt lgkmcnt(0)\n"
      "v_mfma_f32_16x16x32_bf16 v[188:191], v[168:171], v[96:99], v[188:191]\n"
      "v_mfma_f32_16x16x32_bf16 v[208:211], v[168:171], v[100:103], v[208:211]\n"
      "s_add_u32 s97, s78, 98304\n"
      "s_setprio 0\n"
      "buffer_load_dwordx4 v[112:115], v5, s[60:63], s97 offen sc1\n"
      "s_setprio 1\n"
      "v_mfma_f32_16x16x32_bf16 v[228:231], v[168:171], v[104:107], v[228:231]\n"
      "v_mfma_f32_16x16x32_bf16 v[248:251], v[168:171], v[108:111], v[248:251]\n"
      "s_add_u32 s97, s78, 98304\n"
      "s_setprio 0\n"
      "buffer_load_dwordx4 v[116:119], v5, s[60:63], s97 offen offset:1024 sc1\n"
      "s_setprio 1\n"
      "v_mfma_f32_16x16x32_bf16 v[192:195], v[172:175], v[96:99], v[192:195]\n"
      "v_mfma_f32_16x16x32_bf16 v[212:215], v[172:175], v[100:103], v[212:215]\n"
      "s_add_u32 s97, s78, 98304\n"
      "s_setprio 0\n"
      "buffer_load_dwordx4 v[120:123], v5, s[60:63], s97 offen offset:2048 sc1\n"
      "s_setprio 1\n"
      "v_mfma_f32_16x16x32_bf16 v[232:235], v[172:175], v[104:107], v[232:235]\n"
      "v_mfma_f32_16x16x32_bf16 v[252:255], v[172:175], v[108:111], v[252:255]\n"
      "s_add_u32 s97, s78, 98304\n"
      "s_setprio 0\n"
      "buffer_load_dwordx4 v[124:127], v5, s[60:63], s97 offen offset:3072 sc1\n"
      "s_setprio 1\n"
      "v_mfma_f32_16x16x32_bf16 v[196:199], v[176:179], v[96:99], v[196:199]\n"
      "v_mfma_f32_16x16x32_bf16 v[216:219], v[176:179], v[100:103], v[216:219]\n"
      "s_mov_b32 m0, s10\n"
      "s_add_u32 s97, s78, 131072\n"
      "s_setprio 0\n"
      "buffer_load_dwordx4 v5, s[60:63], s97 offen sc1 lds\n"
      "s_setprio 1\n"
      "v_mfma_f32_16x16x32_bf16 v[236:239], v[176:179], v[104:107], v[236:239]\n"
      "v_mfma_f32_16x16x32_bf16 v[84:87], v[176:179], v[108:111], v[84:87]\n"
      "s_mov_b32 m0, s10\n"
      "s_add_u32 s97, s78, 131072\n"
      "s_setprio 0\n"
      "buffer_load_dwordx4 v5, s[60:63], s97 offen offset:1024 sc1 lds\n"
      "s_setprio 1\n"
      "v_mfma_f32_16x16x32_bf16 v[200:203], v[180:183], v[96:99], v[200:203]\n"
      "v_mfma_f32_16x16x32_bf16 v[220:223], v[180:183], v[100:103], v[220:223]\n"
      "s_mov_b32 m0, s10\n"
      "s_add_u32 s97, s78, 131072\n"
      "s_setprio 0\n"
      "buffer_load_dwordx4 v5, s[60:63], s97 offen offset:2048 sc1 lds\n"
      "s_setprio 1\n"
      "v_mfma_f32_16x16x32_bf16 v[240:243], v[180:183], v[104:107], v[240:243]\n"
      "v_mfma_f32_16x16x32_bf16 v[88:91], v[180:183], v[108:111], v[88:91]\n"
      "s_add_u32 m0, s27, 7168\n"
      "s_add_u32 s97, s78, 131072\n"
      "s_setprio 0\n"
      "buffer_load_dwordx4 v5, s[60:63], s97 offen offset:3072 sc1 lds\n"
      "s_setprio 1\n"
      "v_mfma_f32_16x16x32_bf16 v[204:207], v[184:187], v[96:99], v[204:207]\n"
      "v_mfma_f32_16x16x32_bf16 v[224:227], v[184:187], v[100:103], v[224:227]\n"
      "v_mfma_f32_16x16x32_bf16 v[244:247], v[184:187], v[104:107], v[244:247]\n"
      "v_mfma_f32_16x16x32_bf16 v[92:95], v[184:187], v[108:111], v[92:95]\n"
      "s_cmp_lt_u32 s64, 4\n"
      "s_cselect_b32 s5, 1, 0\n"
      "s_cbranch_scc0 L_nogi\n"
      "s_add_u32 s97, s23, 0\n"
      "buffer_load_dwordx4 v[128:131], v29, s[60:63], s97 offen sc1\n"
      "s_add_u32 s97, s23, 8192\n"
      "buffer_load_dwordx4 v[132:135], v29, s[60:63], s97 offen sc1\n"
      "s_add_u32 s97, s23, 16384\n"
      "buffer_load_dwordx4 v[136:139], v29, s[60:63], s97 offen sc1\n"
      "s_add_u32 s97, s23, 24576\n"
      "buffer_load_dwordx4 v[140:143], v29, s[60:63], s97 offen sc1\n"
      "s_add_u32 s97, s23, 32768\n"
      "buffer_load_dwordx4 v[144:147], v29, s[60:63], s97 offen sc1\n"
      "s_add_u32 s97, s23, 40960\n"
      "buffer_load_dwordx4 v[148:151], v29, s[60:63], s97 offen sc1\n"
      "s_add_u32 s97, s23, 49152\n"
      "buffer_load_dwordx4 v[152:155], v29, s[60:63], s97 offen sc1\n"
      "s_add_u32 s97, s23, 57344\n"
      "buffer_load_dwordx4 v[156:159], v29, s[60:63], s97 offen sc1\n"
      "L_nogi:\n"
      "s_add_u32 s97, s78, 163840\n"
      "s_setprio 0\n"
      "buffer_load_dwordx4 v[96:99], v5, s[60:63], s97 offen sc1\n"
      "s_setprio 1\n"
      "s_setprio 0\n"
      "buffer_load_dwordx4 v[100:103], v5, s[60:63], s97 offen offset:1024 sc1\n"
      "s_setprio 1\n"
      "s_setprio 0\n"
      "buffer_load_dwordx4 v[104:107], v5, s[60:63], s97 offen offset:2048 sc1\n"
      "s_setprio 1\n"
      "s_setprio 0\n"
      "buffer_load_dwordx4 v[108:111], v5, s[60:63], s97 offen offset:3072 sc1\n"
      "s_setprio 1\n"
      "s_add_u32 s97, s79, 245760\n"
      "s_setprio 0\n"
      "buffer_load_dwordx4 v[168:171], v5, s[44:47], s97 offen sc1\n"
      "s_setprio 1\n"
      "s_setprio 0\n"
      "buffer_load_dwordx4 v[172:175], v5, s[44:47], s97 offen offset:1024 sc1\n"
      "s_setprio 1\n"
      "s_setprio 0\n"
      "buffer_load_dwordx4 v[176:179], v5, s[44:47], s97 offen offset:2048 sc1\n"
      "s_setprio 1\n"
      "s_setprio 0\n"
      "buffer_load_dwordx4 v[180:183], v5, s[44:47], s97 offen offset:3072 sc1\n"
      "s_setprio 1\n"
      "s_add_u32 s97, s79, 249856\n"
      "s_setprio 0\n"
      "buffer_load_dwordx4 v[184:187], v5, s[44:47], s97 offen sc1\n"
      "s_setprio 1\n"
      "s_cmp_ge_u32 s64, 4\n"
      "s_cbranch_scc1 L_gdone\n"
      "s_waitcnt vmcnt(9)\n"
      "s_mov_b32 s96, 20000\n"
      "L_gchk:\n"
      "v_xor_b32 v6, s26, v131\n"
      "v_xor_b32 v7, s26, v135\n"
      "v_or_b32 v6, v6, v7\n"
      "v_xor_b32 v7, s26, v139\n"
      "v_xor_b32 v31, s26, v143\n"
      "v_or3_b32 v6, v6, v7, v31\n"
      "v_xor_b32 v7, s26, v147\n"
      "v_xor_b32 v31, s26, v151\n"
      "v_or3_b32 v6, v6, v7, v31\n"
      "v_xor_b32 v7, s26, v155\n"
      "v_xor_b32 v31, s26, v159\n"
      "v_or3_b32 v6, v6, v7, v31\n"
      "v_cmp_ne_u32 vcc, 0, v6\n"
      "s_cmp_eq_u64 vcc, 0\n"
      "s_cbranch_scc1 L_gok\n"
      "s_sub_u32 s96, s96, 1\n"
      "s_cmp_eq_u32 s96, 0\n"
      "s_cbranch_scc1 L_gok\n"
      "s_sleep 4\n"
      "s_add_u32 s97, s23, 0\n"
      "buffer_load_dwordx4 v[128:131], v29, s[60:63], s97 offen sc1\n"
      "s_add_u32 s97, s23, 8192\n"
      "buffer_load_dwordx4 v[132:135], v29, s[60:63], s97 offen sc1\n"
      "s_add_u32 s97, s23, 16384\n"
      "buffer_load_dwordx4 v[136:139], v29, s[60:63], s97 offen sc1\n"
      "s_add_u32 s97, s23, 24576\n"
      "buffer_load_dwordx4 v[140:143], v29, s[60:63], s97 offen sc1\n"
      "s_add_u32 s97, s23, 32768\n"
      "buffer_load_dwordx4 v[144:147], v29, s[60:63], s97 offen sc1\n"
      "s_add_u32 s97, s23, 40960\n"
      "buffer_load_dwordx4 v[148:151], v29, s[60:63], s97 offen sc1\n"
      "s_add_u32 s97, s23, 49152\n"
      "buffer_load_dwordx4 v[152:155], v29, s[60:63], s97 offen sc1\n"
      "s_add_u32 s97, s23, 57344\n"
      "buffer_load_dwordx4 v[156:159], v29, s[60:63], s97 offen sc1\n"
      "s_waitcnt vmcnt(0)\n"
      "s_branch L_gchk\n"
      "L_gok:\n"
      "s_mov_b32 s74, 0x04000c0c\n"
      "s_mov_b32 s75, 0x0706000c\n"
      "v_perm_b32 v131, v130, v129, s74\n"
      "v_perm_b32 v135, v134, v133, s74\n"
      "v_perm_b32 v139, v138, v137, s74\n"
      "v_perm_b32 v143, v142, v141, s74\n"
      "v_perm_b32 v147, v146, v145, s74\n"
      "v_perm_b32 v151, v150, v149, s74\n"
      "v_perm_b32 v155, v154, v153, s74\n"
      "v_perm_b32 v159, v158, v157, s74\n"
      "v_perm_b32 v131, v131, v128, s75\n"
      "v_perm_b32 v135, v135, v132, s75\n"
      "v_perm_b32 v139, v139, v136, s75\n"
      "v_perm_b32 v143, v143, v140, s75\n"
      "v_perm_b32 v147, v147, v144, s75\n"
      "v_perm_b32 v151, v151, v148, s75\n"
      "v_perm_b32 v155, v155, v152, s75\n"
      "v_perm_b32 v159, v159, v156, s75\n"
      "v_add_f32 v128, v128, v132\n"
      "v_add_f32 v129, v129, v133\n"
      "v_add_f32 v130, v130, v134\n"
      "v_add_f32 v131, v131, v135\n"
      "v_add_f32 v136, v136, v140\n"
      "v_add_f32 v137, v137, v141\n"
      "v_add_f32 v138, v138, v142\n"
      "v_add_f32 v139, v139, v143\n"
      "v_add_f32 v144, v144, v148\n"
      "v_add_f32 v145, v145, v149\n"
      "v_add_f32 v146, v146, v150\n"
      "v_add_f32 v147, v147, v151\n"
      "v_add_f32 v152, v152, v156\n"
      "v_add_f32 v153, v153, v157\n"
      "v_add_f32 v154, v154, v158\n"
      "v_add_f32 v155, v155, v159\n"
      "v_add_f32 v128, v128, v136\n"
      "v_add_f32 v129, v129, v137\n"
      "v_add_f32 v130, v130, v138\n"
      "v_add_f32 v131, v131, v139\n"
      "v_add_f32 v144, v144, v152\n"
      "v_add_f32 v145, v145, v153\n"
      "v_add_f32 v146, v146, v154\n"
      "v_add_f32 v147, v147, v155\n"
      "v_add_f32 v128, v128, v144\n"
      "v_add_f32 v129, v129, v145\n"
      "v_add_f32 v130, v130, v146\n"
      "v_add_f32 v131, v131, v147\n"
      "v_cvt_pk_bf16_f32 v6, v128, v129\n"
      "v_cvt_pk_bf16_f32 v7, v130, v131\n"
      "ds_write_b64 v21, v[6:7]\n"
      "L_gdone:\n"
      "s_waitcnt lgkmcnt(0)\n"
      "s_barrier\n"
      "ds_read_b128 v[128:131], v22 offset:0\n"
      "ds_read_b128 v[132:135], v22 offset:16\n"
      "ds_read_b128 v[136:139], v22 offset:32\n"
      "ds_read_b128 v[140:143], v22 offset:48\n"
      "ds_read_b128 v[144:147], v22 offset:64\n"
      "s_waitcnt lgkmcnt(4)\n"
      "v_mfma_f32_32x32x16_bf16 v[148:163], v[128:131], v[64:67], 0\n"
      "s_waitcnt lgkmcnt(3)\n"
      "v_mfma_f32_32x32x16_bf16 v[148:163], v[132:135], v[68:71], v[148:163]\n"
      "s_waitcnt lgkmcnt(2)\n"
      "v_mfma_f32_32x32x16_bf16 v[148:163], v[136:139], v[72:75], v[148:163]\n"
      "s_waitcnt lgkmcnt(1)\n"
      "v_mfma_f32_32x32x16_bf16 v[148:163], v[140:143], v[76:79], v[148:163]\n"
      "s_waitcnt lgkmcnt(0)\n"
      "v_mfma_f32_32x32x16_bf16 v[148:163], v[144:147], v[80:83], v[148:163]\n"
      "ds_read_b128 v[128:131], v20 offset:0\n"
      "ds_read_b128 v[132:135], v20 offset:1024\n"
      "ds_read_b128 v[136:139], v20 offset:2048\n"
      "ds_read_b128 v[140:143], v20 offset:3072\n"
      "ds_read_b128 v[144:147], v20 offset:4096\n"
      "v_lshlrev_b32 v29, 3, v0\n"
      "v_add_u32 v16, 61440, v25\n"
      "v_add_u32 v17, 122880, v25\n"
      "s_nop 3\n"
      "v_add_f32 v160, 0x358637bd, v160\n"
      "v_rcp_f32 v160, v160\n"
      "s_nop 0\n"
      "v_mul_f32 v160, v160, v28\n"
      "v_mul_f32 v148, v148, v160\n"
      "v_mul_f32 v149, v149, v160\n"
      "v_mul_f32 v150, v150, v160\n"
      "v_mul_f32 v151, v151, v160\n"
      "v_mul_f32 v152, v152, v160\n"
      "v_mul_f32 v153, v153, v160\n"
      "v_mul_f32 v154, v154, v160\n"
      "v_mul_f32 v155, v155, v160\n"
      "v_mul_f32 v156, v156, v160\n"
      "v_mul_f32 v157, v157, v160\n"
      "v_mul_f32 v158, v158, v160\n"
      "v_mul_f32 v159, v159, v160\n"
      "v_cvt_pk_bf16_f32 v56, v148, v149\n"
      "v_cvt_pk_bf16_f32 v57, v150, v151\n"
      "v_cvt_pk_bf16_f32 v58, v152, v153\n"
      "v_cvt_pk_bf16_f32 v59, v154, v155\n"
      "v_cvt_pk_bf16_f32 v60, v156, v157\n"
      "v_cvt_pk_bf16_f32 v61, v158, v159\n"
      "buffer_store_dwordx4 v[56:59], v1, s[60:63], s24 offen sc1\n"
      "s_mov_b32 exec_hi, 0\n"
      "buffer_store_dwordx2 v[60:61], v23, s[60:63], s25 offen sc1\n"
      "s_mov_b32 exec_hi, -1\n"
      "s_waitcnt vmcnt(19)\n"
      "ds_read_b128 v[148:151], v20 offset:5120\n"
      "ds_read_b128 v[152:155], v20 offset:6144\n"
      "ds_read_b128 v[156:159], v20 offset:7168\n"
      "ds_read_b128 v[160:163], v20 offset:8192\n"
      "ds_read_b128 v[164:167], v20 offset:9216\n"
      "s_waitcnt vmcnt(15)\n"
      "v_mfma_f32_16x16x32_bf16 v[188:191], v[32:35], v[112:115], v[188:191]\n"
      "v_mfma_f32_16x16x32_bf16 v[208:211], v[32:35], v[116:119], v[208:211]\n"
      "v_mfma_f32_16x16x32_bf16 v[228:231], v[32:35], v[120:123], v[228:231]\n"
      "v_mfma_f32_16x16x32_bf16 v[248:251], v[32:35], v[124:127], v[248:251]\n"
      "v_mfma_f32_16x16x32_bf16 v[192:195], v[36:39], v[112:115], v[192:195]\n"
      "v_mfma_f32_16x16x32_bf16 v[212:215], v[36:39], v[116:119], v[212:215]\n"
      "v_mfma_f32_16x16x32_bf16 v[232:235], v[36:39], v[120:123], v[232:235]\n"
      "v_mfma_f32_16x16x32_bf16 v[252:255], v[36:39], v[124:127], v[252:255]\n"
      "v_mfma_f32_16x16x32_bf16 v[196:199], v[40:43], v[112:115], v[196:199]\n"
      "v_mfma_f32_16x16x32_bf16 v[216:219], v[40:43], v[116:119], v[216:219]\n"
      "v_mfma_f32_16x16x32_bf16 v[236:239], v[40:43], v[120:123], v[236:239]\n"
      "v_mfma_f32_16x16x32_bf16 v[84:87], v[40:43], v[124:127], v[84:87]\n"
      "v_mfma_f32_16x16x32_bf16 v[200:203], v[44:47], v[112:115], v[200:203]\n"
      "v_mfma_f32_16x16x32_bf16 v[220:223], v[44:47], v[116:119], v[220:223]\n"
      "v_mfma_f32_16x16x32_bf16 v[240:243], v[44:47], v[120:123], v[240:243]\n"
      "v_mfma_f32_16x16x32_bf16 v[88:91], v[44:47], v[124:127], v[88:91]\n"
      "v_mfma_f32_16x16x32_bf16 v[204:207], v[48:51], v[112:115], v[204:207]\n"
      "v_mfma_f32_16x16x32_bf16 v[224:227], v[48:51], v[116:119], v[224:227]\n"
      "v_mfma_f32_16x16x32_bf16 v[244:247], v[48:51], v[120:123], v[244:247]\n"
      "v_mfma_f32_16x16x32_bf16 v[92:95], v[48:51], v[124:127], v[92:95]\n"
      "s_waitcnt vmcnt(11)\n"
      "ds_read_b128 v[112:115], v27 offset:0\n"
      "ds_read_b128 v[116:119], v27 offset:1024\n"
      "ds_read_b128 v[120:123], v27 offset:2048\n"
      "ds_read_b128 v[124:127], v20 offset:10240\n"
      "s_waitcnt vmcnt(0)\n"
      "s_mov_b64 exec, 1\n"
      "buffer_store_dword v2, off, s[60:63], s40 sc1\n"
      "s_mov_b64 exec, -1\n"
      "s_waitcnt lgkmcnt(4)\n"
      "v_mfma_f32_16x16x32_bf16 v[188:191], v[148:151], v[96:99], v[188:191]\n"
      "v_mfma_f32_16x16x32_bf16 v[208:211], v[148:151], v[100:103], v[208:211]\n"
      "s_add_u32 s97, s78, 196608\n"
      "s_setprio 0\n"
      "buffer_load_dwordx4 v[32:35], v5, s[44:47], s97 offen sc1\n"
      "s_setprio 1\n"
      "v_mfma_f32_16x16x32_bf16 v[228:231], v[148:151], v[104:107], v[228:231]\n"
      "v_mfma_f32_16x16x32_bf16 v[248:251], v[148:151], v[108:111], v[248:251]\n"
      "s_add_u32 s97, s78, 196608\n"
      "s_setprio 0\n"
      "buffer_load_dwordx4 v[36:39], v5, s[44:47], s97 offen offset:1024 sc1\n"
      "s_setprio 1\n"
      "v_mfma_f32_16x16x32_bf16 v[192:195], v[152:155], v[96:99], v[192:195]\n"
      "v_mfma_f32_16x16x32_bf16 v[212:215], v[152:155], v[100:103], v[212:215]\n"
      "s_add_u32 s97, s78, 196608\n"
      "s_setprio 0\n"
      "buffer_load_dwordx4 v[40:43], v5, s[44:47], s97 offen offset:2048 sc1\n"
      "s_setprio 1\n"
      "v_mfma_f32_16x16x32_bf16 v[232:235], v[152:155], v[104:107], v[232:235]\n"
      "v_mfma_f32_16x16x32_bf16 v[252:255], v[152:155], v[108:111], v[252:255]\n"
      "s_add_u32 s97, s78, 196608\n"
      "s_setprio 0\n"
      "buffer_load_dwordx4 v[44:47], v5, s[44:47], s97 offen offset:3072 sc1\n"
      "s_setprio 1\n"
      "v_mfma_f32_16x16x32_bf16 v[196:199], v[156:159], v[96:99], v[196:199]\n"
      "v_mfma_f32_16x16x32_bf16 v[216:219], v[156:159], v[100:103], v[216:219]\n"
      "s_add_u32 s97, s41, 0\n"
      "s_setprio 0\n"
      "buffer_load_dwordx4 v[64:67], v5, s[60:63], s97 offen sc1\n"
      "s_setprio 1\n"
      "v_mfma_f32_16x16x32_bf16 v[236:239], v[156:159], v[104:107], v[236:239]\n"
      "v_mfma_f32_16x16x32_bf16 v[84:87], v[156:159], v[108:111], v[84:87]\n"
      "s_add_u32 s97, s41, 0\n"
      "s_setprio 0\n"
      "buffer_load_dwordx4 v[68:71], v5, s[60:63], s97 offen offset:1024 sc1\n"
      "s_setprio 1\n"
      "v_mfma_f32_16x16x32_bf16 v[200:203], v[160:163], v[96:99], v[200:203]\n"
      "v_mfma_f32_16x16x32_bf16 v[220:223], v[160:163], v[100:103], v[220:223]\n"
      "s_add_u32 s97, s41, 0\n"
      "s_setprio 0\n"
      "buffer_load_dwordx4 v[72:75], v5, s[60:63], s97 offen offset:2048 sc1\n"
      "s_setprio 1\n"
      "v_mfma_f32_16x16x32_bf16 v[240:243], v[160:163], v[104:107], v[240:243]\n"
      "v_mfma_f32_16x16x32_bf16 v[88:91], v[160:163], v[108:111], v[88:91]\n"
      "s_add_u32 s97, s41, 0\n"
      "s_setprio 0\n"
      "buffer_load_dwordx4 v[76:79], v5, s[60:63], s97 offen offset:3072 sc1\n"
      "s_setprio 1\n"
      "v_mfma_f32_16x16x32_bf16 v[204:207], v[164:167], v[96:99], v[204:207]\n"
      "v_mfma_f32_16x16x32_bf16 v[224:227], v[164:167], v[100:103], v[224:227]\n"
      "s_add_u32 s97, s41, 4096\n"
      "s_setprio 0\n"
      "buffer_load_dwordx4 v[80:83], v5, s[60:63], s97 offen sc1\n"
      "s_setprio 1\n"
      "v_mfma_f32_16x16x32_bf16 v[244:247], v[164:167], v[104:107], v[244:247]\n"
      "v_mfma_f32_16x16x32_bf16 v[92:95], v[164:167], v[108:111], v[92:95]\n"
      "s_waitcnt lgkmcnt(0)\n"
      "v_mfma_f32_16x16x32_bf16 v[188:191], v[128:131], v[112:115], v[188:191]\n"
      "v_mfma_f32_16x16x32_bf16 v[208:211], v[128:131], v[116:119], v[208:211]\n"
      "v_mfma_f32_16x16x32_bf16 v[228:231], v[128:131], v[120:123], v[228:231]\n"
      "v_mfma_f32_16x16x32_bf16 v[248:251], v[128:131], v[124:127], v[248:251]\n"
      "v_mfma_f32_16x16x32_bf16 v[192:195], v[132:135], v[112:115], v[192:195]\n"
      "v_mfma_f32_16x16x32_bf16 v[212:215], v[132:135], v[116:119], v[212:215]\n"
      "v_mfma_f32_16x16x32_bf16 v[232:235], v[132:135], v[120:123], v[232:235]\n"
      "v_mfma_f32_16x16x32_bf16 v[252:255], v[132:135], v[124:127], v[252:255]\n"
      "v_mfma_f32_16x16x32_bf16 v[196:199], v[136:139], v[112:115], v[196:199]\n"
      "v_mfma_f32_16x16x32_bf16 v[216:219], v[136:139], v[116:119], v[216:219]\n"
      "v_mfma_f32_16x16x32_bf16 v[236:239], v[136:139], v[120:123], v[236:239]\n"
      "v_mfma_f32_16x16x32_bf16 v[84:87], v[136:139], v[124:127], v[84:87]\n"
      "v_mfma_f32_16x16x32_bf16 v[200:203], v[140:143], v[112:115], v[200:203]\n"
      "v_mfma_f32_16x16x32_bf16 v[220:223], v[140:143], v[116:119], v[220:223]\n"
      "v_mfma_f32_16x16x32_bf16 v[240:243], v[140:143], v[120:123], v[240:243]\n"
      "v_mfma_f32_16x16x32_bf16 v[88:91], v[140:143], v[124:127], v[88:91]\n"
      "v_mfma_f32_16x16x32_bf16 v[204:207], v[144:147], v[112:115], v[204:207]\n"
      "v_mfma_f32_16x16x32_bf16 v[224:227], v[144:147], v[116:119], v[224:227]\n"
      "v_mfma_f32_16x16x32_bf16 v[244:247], v[144:147], v[120:123], v[244:247]\n"
      "v_mfma_f32_16x16x32_bf16 v[92:95], v[144:147], v[124:127], v[92:95]\n"
      "s_waitcnt vmcnt(5)\n"
      "v_mfma_f32_16x16x32_bf16 v[188:191], v[168:171], v[32:35], v[188:191]\n"
      "v_mfma_f32_16x16x32_bf16 v[208:211], v[168:171], v[36:39], v[208:211]\n"
      "s_add_u32 s97, s41, 5120\n"
      "s_setprio 0\n"
      "buffer_load_dwordx4 v[128:131], v5, s[60:63], s97 offen sc1\n"
      "s_setprio 1\n"
      "v_mfma_f32_16x16x32_bf16 v[228:231], v[168:171], v[40:43], v[228:231]\n"
      "v_mfma_f32_16x16x32_bf16 v[248:251], v[168:171], v[44:47], v[248:251]\n"
      "s_add_u32 s97, s41, 5120\n"
      "s_setprio 0\n"
      "buffer_load_dwordx4 v[132:135], v5, s[60:63], s97 offen offset:1024 sc1\n"
      "s_setprio 1\n"
      "v_mfma_f32_16x16x32_bf16 v[192:195], v[172:175], v[32:35], v[192:195]\n"
      "v_mfma_f32_16x16x32_bf16 v[212:215], v[172:175], v[36:39], v[212:215]\n"
      "s_add_u32 s97, s41, 5120\n"
      "s_setprio 0\n"
      "buffer_load_dwordx4 v[136:139], v5, s[60:63], s97 offen offset:2048 sc1\n"
      "s_setprio 1\n"
      "v_mfma_f32_16x16x32_bf16 v[232:235], v[172:175], v[40:43], v[232:235]\n"
      "v_mfma_f32_16x16x32_bf16 v[252:255], v[172:175], v[44:47], v[252:255]\n"
      "s_add_u32 s97, s41, 5120\n"
      "s_setprio 0\n"
      "buffer_load_dwordx4 v[140:143], v5, s[60:63], s97 offen offset:3072 sc1\n"
      "s_setprio 1\n"
      "v_mfma_f32_16x16x32_bf16 v[196:199], v[176:179], v[32:35], v[196:199]\n"
      "v_mfma_f32_16x16x32_bf16 v[216:219], v[176:179], v[36:39], v[216:219]\n"
      "s_add_u32 s97, s41, 9216\n"
      "s_setprio 0\n"
      "buffer_load_dwordx4 v[144:147], v5, s[60:63], s97 offen sc1\n"
      "s_setprio 1\n"
      "v_mfma_f32_16x16x32_bf16 v[236:239], v[176:179], v[40:43], v[236:239]\n"
      "v_mfma_f32_16x16x32_bf16 v[84:87], v[176:179], v[44:47], v[84:87]\n"
      "v_mfma_f32_16x16x32_bf16 v[200:203], v[180:183], v[32:35], v[200:203]\n"
      "v_mfma_f32_16x16x32_bf16 v[220:223], v[180:183], v[36:39], v[220:223]\n"
      "v_mfma_f32_16x16x32_bf16 v[240:243], v[180:183], v[40:43], v[240:243]\n"
      "v_mfma_f32_16x16x32_bf16 v[88:91], v[180:183], v[44:47], v[88:91]\n"
      "v_mfma_f32_16x16x32_bf16 v[204:207], v[184:187], v[32:35], v[204:207]\n"
      "v_mfma_f32_16x16x32_bf16 v[224:227], v[184:187], v[36:39], v[224:227]\n"
      "v_mfma_f32_16x16x32_bf16 v[244:247], v[184:187], v[40:43], v[244:247]\n"
      "v_mfma_f32_16x16x32_bf16 v[92:95], v[184:187], v[44:47], v[92:95]\n"
      "s_cmp_lt_u32 s54, 4\n"
      "s_cbranch_scc1 L_nowo2\n"
      "s_add_u32 s97, s42, 0\n"
      "s_setprio 0\n"
      "buffer_load_dwordx4 v[148:151], v5, s[60:63], s97 offen sc1\n"
      "s_setprio 1\n"
      "s_setprio 0\n"
      "buffer_load_dwordx4 v[152:155], v5, s[60:63], s97 offen offset:1024 sc1\n"
      "s_setprio 1\n"
      "s_setprio 0\n"
      "buffer_load_dwordx4 v[156:159], v5, s[60:63], s97 offen offset:2048 sc1\n"
      "s_setprio 1\n"
      "s_setprio 0\n"
      "buffer_load_dwordx4 v[160:163], v5, s[60:63], s97 offen offset:3072 sc1\n"
      "s_setprio 1\n"
      "s_add_u32 s97, s42, 4096\n"
      "s_setprio 0\n"
      "buffer_load_dwordx4 v[164:167], v5, s[60:63], s97 offen sc1\n"
      "s_setprio 1\n"
      "L_nowo2:\n"
      "v_pk_mul_f32 v[188:189], v[188:189], v[8:9] op_sel_hi:[1,0]\n"
      "v_pk_mul_f32 v[190:191], v[190:191], v[8:9] op_sel_hi:[1,0]\n"
      "v_pk_mul_f32 v[208:209], v[208:209], v[10:11] op_sel_hi:[1,0]\n"
      "v_pk_mul_f32 v[210:211], v[210:211], v[10:11] op_sel_hi:[1,0]\n"
      "v_pk_mul_f32 v[228:229], v[228:229], v[12:13] op_sel_hi:[1,0]\n"
      "v_pk_mul_f32 v[230:231], v[230:231], v[12:13] op_sel_hi:[1,0]\n"
      "v_pk_mul_f32 v[248:249], v[248:249], v[14:15] op_sel_hi:[1,0]\n"
      "v_pk_mul_f32 v[250:251], v[250:251], v[14:15] op_sel_hi:[1,0]\n"
      "v_pk_mul_f32 v[192:193], v[192:193], v[8:9] op_sel_hi:[1,0]\n"
      "v_pk_mul_f32 v[194:195], v[194:195], v[8:9] op_sel_hi:[1,0]\n"
      "v_pk_mul_f32 v[212:213], v[212:213], v[10:11] op_sel_hi:[1,0]\n"
      "v_pk_mul_f32 v[214:215], v[214:215], v[10:11] op_sel_hi:[1,0]\n"
      "v_pk_mul_f32 v[232:233], v[232:233], v[12:13] op_sel_hi:[1,0]\n"
      "v_pk_mul_f32 v[234:235], v[234:235], v[12:13] op_sel_hi:[1,0]\n"
      "v_pk_mul_f32 v[252:253], v[252:253], v[14:15] op_sel_hi:[1,0]\n"
      "v_pk_mul_f32 v[254:255], v[254:255], v[14:15] op_sel_hi:[1,0]\n"
      "v_pk_mul_f32 v[196:197], v[196:197], v[8:9] op_sel_hi:[1,0]\n"
      "v_pk_mul_f32 v[198:199], v[198:199], v[8:9] op_sel_hi:[1,0]\n"
      "v_pk_mul_f32 v[216:217], v[216:217], v[10:11] op_sel_hi:[1,0]\n"
      "v_pk_mul_f32 v[218:219], v[218:219], v[10:11] op_sel_hi:[1,0]\n"
      "v_pk_mul_f32 v[236:237], v[236:237], v[12:13] op_sel_hi:[1,0]\n"
      "v_pk_mul_f32 v[238:239], v[238:239], v[12:13] op_sel_hi:[1,0]\n"
      "v_pk_mul_f32 v[84:85], v[84:85], v[14:15] op_sel_hi:[1,0]\n"
      "v_pk_mul_f32 v[86:87], v[86:87], v[14:15] op_sel_hi:[1,0]\n"
      "v_pk_mul_f32 v[200:201], v[200:201], v[8:9] op_sel_hi:[1,0]\n"
      "v_pk_mul_f32 v[202:203], v[202:203], v[8:9] op_sel_hi:[1,0]\n"
      "v_pk_mul_f32 v[220:221], v[220:221], v[10:11] op_sel_hi:[1,0]\n"
      "v_pk_mul_f32 v[222:223], v[222:223], v[10:11] op_sel_hi:[1,0]\n"
      "v_pk_mul_f32 v[240:241], v[240:241], v[12:13] op_sel_hi:[1,0]\n"
      "v_pk_mul_f32 v[242:243], v[242:243], v[12:13] op_sel_hi:[1,0]\n"
      "v_pk_mul_f32 v[88:89], v[88:89], v[14:15] op_sel_hi:[1,0]\n"
      "v_pk_mul_f32 v[90:91], v[90:91], v[14:15] op_sel_hi:[1,0]\n"
      "v_pk_mul_f32 v[204:205], v[204:205], v[8:9] op_sel_hi:[1,0]\n"
      "v_pk_mul_f32 v[206:207], v[206:207], v[8:9] op_sel_hi:[1,0]\n"
      "v_pk_mul_f32 v[224:225], v[224:225], v[10:11] op_sel_hi:[1,0]\n"
      "v_pk_mul_f32 v[226:227], v[226:227], v[10:11] op_sel_hi:[1,0]\n"
      "v_pk_mul_f32 v[244:245], v[244:245], v[12:13] op_sel_hi:[1,0]\n"
      "v_pk_mul_f32 v[246:247], v[246:247], v[12:13] op_sel_hi:[1,0]\n"
      "v_pk_mul_f32 v[92:93], v[92:93], v[14:15] op_sel_hi:[1,0]\n"
      "v_pk_mul_f32 v[94:95], v[94:95], v[14:15] op_sel_hi:[1,0]\n"
      "s_cmp_lg_u32 s64, 7\n"
      "s_cbranch_scc1 Lai_19\n"
      "s_lshl_b32 s33, s65, 2\n"
      "s_add_u32 s33, s33, s70\n"
      "s_lshl_b32 s33, s33, 8\n"
      "s_add_u32 s33, s33, 20480\n"
      "v_min_u32 v6, 63, v0\n"
      "v_lshlrev_b32 v6, 2, v6\n"
      "buffer_load_dword v7, v6, s[60:63], s33 offen sc1\n"
      "Lai_19:\n"
      "s_cmp_lg_u32 s64, 7\n"
      "s_cbranch_scc1 Lacd_22\n"
      "s_waitcnt vmcnt(0)\n"
      "s_mov_b32 s96, 20000\n"
      "Lac_21:\n"
      "v_cmp_ne_u32 vcc, s26, v7\n"
      "s_cmp_eq_u64 vcc, 0\n"
      "s_cbranch_scc1 Lacd_22\n"
      "s_sub_u32 s96, s96, 1\n"
      "s_cmp_eq_u32 s96, 0\n"
      "s_cbranch_scc1 Lacd_22\n"
      "s_sleep 4\n"
      "buffer_load_dword v7, v6, s[60:63], s33 offen sc1\n"
      "s_waitcnt vmcnt(0)\n"
      "s_branch Lac_21\n"
      "Lacd_22:\n"
      "s_barrier\n"
      "s_add_u32 s97, s43, 0\n"
      "s_setprio 0\n"
      "buffer_load_dwordx4 v[96:99], v5, s[60:63], s97 offen sc1\n"
      "s_setprio 1\n"
      "s_setprio 0\n"
      "buffer_load_dwordx4 v[100:103], v5, s[60:63], s97 offen offset:1024 sc1\n"
      "s_setprio 1\n"
      "s_setprio 0\n"
      "buffer_load_dwordx4 v[104:107], v5, s[60:63], s97 offen offset:2048 sc1\n"
      "s_setprio 1\n"
      "s_setprio 0\n"
      "buffer_load_dwordx4 v[108:111], v5, s[60:63], s97 offen offset:3072 sc1\n"
      "s_setprio 1\n"
      "s_add_u32 s97, s43, 4096\n"
      "s_setprio 0\n"
      "buffer_load_dwordx4 v[112:115], v5, s[60:63], s97 offen sc1\n"
      "s_setprio 1\n"
      "s_setprio 0\n"
      "buffer_load_dwordx4 v[116:119], v5, s[60:63], s97 offen offset:1024 sc1\n"
      "s_setprio 1\n"
      "s_setprio 0\n"
      "buffer_load_dwordx4 v[120:123], v5, s[60:63], s97 offen offset:2048 sc1\n"
      "s_setprio 1\n"
      "s_setprio 0\n"
      "buffer_load_dwordx4 v[124:127], v5, s[60:63], s97 offen offset:3072 sc1\n"
      "s_setprio 1\n"
      "s_cmp_ge_u32 s54, 4\n"
      "s_cselect_b32 s5, 1, 0\n"
      "s_cbranch_scc0 L_nox2\n"
      "buffer_load_dwordx2 v[32:33], v29, s[60:63], s48 offen offset:0 sc1\n"
      "buffer_load_dwordx2 v[34:35], v29, s[60:63], s48 offen offset:512 sc1\n"
      "buffer_load_dwordx2 v[36:37], v29, s[60:63], s48 offen offset:1024 sc1\n"
      "buffer_load_dwordx2 v[38:39], v29, s[60:63], s48 offen offset:1536 sc1\n"
      "buffer_load_dwordx2 v[40:41], v29, s[60:63], s48 offen offset:2048 sc1\n"
      "buffer_load_dwordx2 v[42:43], v29, s[60:63], s48 offen offset:2560 sc1\n"
      "buffer_load_dwordx2 v[44:45], v29, s[60:63], s48 offen offset:3072 sc1\n"
      "buffer_load_dwordx2 v[46:47], v29, s[60:63], s48 offen offset:3584 sc1\n"
      "L_nox2:\n"
      "s_cmp_lg_u32 s5, 0\n"
      "s_cbranch_scc1 Lxw_23\n"
      "s_waitcnt vmcnt(4)\n"
      "s_branch Lxj_24\n"
      "Lxw_23:\n"
      "s_waitcnt vmcnt(12)\n"
      "Lxj_24:\n"
      "v_mfma_f32_16x16x32_bf16 v[188:191], v[64:67], v[96:99], v[188:191]\n"
      "v_mfma_f32_16x16x32_bf16 v[208:211], v[64:67], v[100:103], v[208:211]\n"
      "v_mfma_f32_16x16x32_bf16 v[228:231], v[64:67], v[104:107], v[228:231]\n"
      "v_mfma_f32_16x16x32_bf16 v[248:251], v[64:67], v[108:111], v[248:251]\n"
      "v_mfma_f32_16x16x32_bf16 v[192:195], v[68:71], v[96:99], v[192:195]\n"
      "v_mfma_f32_16x16x32_bf16 v[212:215], v[68:71], v[100:103], v[212:215]\n"
      "v_mfma_f32_16x16x32_bf16 v[232:235], v[68:71], v[104:107], v[232:235]\n"
      "v_mfma_f32_16x16x32_bf16 v[252:255], v[68:71], v[108:111], v[252:255]\n"
      "v_mfma_f32_16x16x32_bf16 v[196:199], v[72:75], v[96:99], v[196:199]\n"
      "v_mfma_f32_16x16x32_bf16 v[216:219], v[72:75], v[100:103], v[216:219]\n"
      "v_mfma_f32_16x16x32_bf16 v[236:239], v[72:75], v[104:107], v[236:239]\n"
      "v_mfma_f32_16x16x32_bf16 v[84:87], v[72:75], v[108:111], v[84:87]\n"
      "v_mfma_f32_16x16x32_bf16 v[200:203], v[76:79], v[96:99], v[200:203]\n"
      "v_mfma_f32_16x16x32_bf16 v[220:223], v[76:79], v[100:103], v[220:223]\n"
      "v_mfma_f32_16x16x32_bf16 v[240:243], v[76:79], v[104:107], v[240:243]\n"
      "v_mfma_f32_16x16x32_bf16 v[88:91], v[76:79], v[108:111], v[88:91]\n"
      "v_mfma_f32_16x16x32_bf16 v[204:207], v[80:83], v[96:99], v[204:207]\n"
      "v_mfma_f32_16x16x32_bf16 v[224:227], v[80:83], v[100:103], v[224:227]\n"
      "v_mfma_f32_16x16x32_bf16 v[244:247], v[80:83], v[104:107], v[244:247]\n"
      "v_mfma_f32_16x16x32_bf16 v[92:95], v[80:83], v[108:111], v[92:95]\n"
      "s_cmp_lt_u32 s54, 4\n"
      "s_cbranch_scc1 L_noa2\n"
      "s_waitcnt vmcnt(0)\n"
      "v_mfma_f32_16x16x32_bf16 v[188:191], v[148:151], v[32:35], v[188:191]\n"
      "v_mfma_f32_16x16x32_bf16 v[208:211], v[148:151], v[36:39], v[208:211]\n"
      "v_mfma_f32_16x16x32_bf16 v[228:231], v[148:151], v[40:43], v[228:231]\n"
      "v_mfma_f32_16x16x32_bf16 v[248:251], v[148:151], v[44:47], v[248:251]\n"
      "v_mfma_f32_16x16x32_bf16 v[192:195], v[152:155], v[32:35], v[192:195]\n"
      "v_mfma_f32_16x16x32_bf16 v[212:215], v[152:155], v[36:39], v[212:215]\n"
      "v_mfma_f32_16x16x32_bf16 v[232:235], v[152:155], v[40:43], v[232:235]\n"
      "v_mfma_f32_16x16x32_bf16 v[252:255], v[152:155], v[44:47], v[252:255]\n"
      "v_mfma_f32_16x16x32_bf16 v[196:199], v[156:159], v[32:35], v[196:199]\n"
      "v_mfma_f32_16x16x32_bf16 v[216:219], v[156:159], v[36:39], v[216:219]\n"
      "v_mfma_f32_16x16x32_bf16 v[236:239], v[156:159], v[40:43], v[236:239]\n"
      "v_mfma_f32_16x16x32_bf16 v[84:87], v[156:159], v[44:47], v[84:87]\n"
      "v_mfma_f32_16x16x32_bf16 v[200:203], v[160:163], v[32:35], v[200:203]\n"
      "v_mfma_f32_16x16x32_bf16 v[220:223], v[160:163], v[36:39], v[220:223]\n"
      "v_mfma_f32_16x16x32_bf16 v[240:243], v[160:163], v[40:43], v[240:243]\n"
      "v_mfma_f32_16x16x32_bf16 v[88:91], v[160:163], v[44:47], v[88:91]\n"
      "v_mfma_f32_16x16x32_bf16 v[204:207], v[164:167], v[32:35], v[204:207]\n"
      "v_mfma_f32_16x16x32_bf16 v[224:227], v[164:167], v[36:39], v[224:227]\n"
      "v_mfma_f32_16x16x32_bf16 v[244:247], v[164:167], v[40:43], v[244:247]\n"
      "v_mfma_f32_16x16x32_bf16 v[92:95], v[164:167], v[44:47], v[92:95]\n"
      "L_noa2:\n"
      "s_waitcnt vmcnt(0)\n"
      "v_mfma_f32_16x16x32_bf16 v[188:191], v[128:131], v[112:115], v[188:191]\n"
      "v_mfma_f32_16x16x32_bf16 v[208:211], v[128:131], v[116:119], v[208:211]\n"
      "v_mfma_f32_16x16x32_bf16 v[228:231], v[128:131], v[120:123], v[228:231]\n"
      "v_mfma_f32_16x16x32_bf16 v[248:251], v[128:131], v[124:127], v[248:251]\n"
      "v_mfma_f32_16x16x32_bf16 v[192:195], v[132:135], v[112:115], v[192:195]\n"
      "s_nop 3\n"
      "ds_write_b128 v24, v[188:191] offset:0\n"
      "v_mfma_f32_16x16x32_bf16 v[212:215], v[132:135], v[116:119], v[212:215]\n"
      "ds_write_b128 v24, v[208:211] offset:5120\n"
      "v_mfma_f32_16x16x32_bf16 v[232:235], v[132:135], v[120:123], v[232:235]\n"
      "ds_write_b128 v24, v[228:231] offset:10240\n"
      "v_mfma_f32_16x16x32_bf16 v[252:255], v[132:135], v[124:127], v[252:255]\n"
      "ds_write_b128 v24, v[248:251] offset:15360\n"
      "v_mfma_f32_16x16x32_bf16 v[196:199], v[136:139], v[112:115], v[196:199]\n"
      "ds_write_b128 v24, v[192:195] offset:1024\n"
      "v_mfma_f32_16x16x32_bf16 v[216:219], v[136:139], v[116:119], v[216:219]\n"
      "ds_write_b128 v24, v[212:215] offset:6144\n"
      "v_mfma_f32_16x16x32_bf16 v[236:239], v[136:139], v[120:123], v[236:239]\n"
      "ds_write_b128 v24, v[232:235] offset:11264\n"
      "v_mfma_f32_16x16x32_bf16 v[84:87], v[136:139], v[124:127], v[84:87]\n"
      "ds_write_b128 v24, v[252:255] offset:16384\n"
      "v_mfma_f32_16x16x32_bf16 v[200:203], v[140:143], v[112:115], v[200:203]\n"
      "ds_write_b128 v24, v[196:199] offset:2048\n"
      "v_mfma_f32_16x16x32_bf16 v[220:223], v[140:143], v[116:119], v[220:223]\n"
      "ds_write_b128 v24, v[216:219] offset:7168\n"
      "v_mfma_f32_16x16x32_bf16 v[240:243], v[140:143], v[120:123], v[240:243]\n"
      "ds_write_b128 v24, v[236:239] offset:12288\n"
      "v_mfma_f32_16x16x32_bf16 v[88:91], v[140:143], v[124:127], v[88:91]\n"
      "ds_write_b128 v24, v[84:87] offset:17408\n"
      "v_mfma_f32_16x16x32_bf16 v[204:207], v[144:147], v[112:115], v[204:207]\n"
      "ds_write_b128 v24, v[200:203] offset:3072\n"
      "v_mfma_f32_16x16x32_bf16 v[224:227], v[144:147], v[116:119], v[224:227]\n"
      "ds_write_b128 v24, v[220:223] offset:8192\n"
      "v_mfma_f32_16x16x32_bf16 v[244:247], v[144:147], v[120:123], v[244:247]\n"
      "ds_write_b128 v24, v[240:243] offset:13312\n"
      "v_mfma_f32_16x16x32_bf16 v[92:95], v[144:147], v[124:127], v[92:95]\n"
      "ds_write_b128 v24, v[88:91] offset:18432\n"
      "s_nop 0\n"
      "ds_write_b128 v24, v[204:207] offset:4096\n"
      "s_nop 0\n"
      "ds_write_b128 v24, v[224:227] offset:9216\n"
      "s_nop 0\n"
      "ds_write_b128 v24, v[244:247] offset:14336\n"
      "s_nop 0\n"
      "ds_write_b128 v24, v[92:95] offset:19456\n"
      "s_waitcnt lgkmcnt(0)\n"
      "s_barrier\n"
      "ds_read_b128 v[96:99], v25 offset:0\n"
      "ds_read_b128 v[100:103], v25 offset:20480\n"
      "ds_read_b128 v[104:107], v25 offset:40960\n"
      "ds_read_b128 v[108:111], v16 offset:0\n"
      "ds_read_b128 v[112:115], v16 offset:20480\n"
      "ds_read_b128 v[116:119], v16 offset:40960\n"
      "ds_read_b128 v[120:123], v17 offset:0\n"
      "ds_read_b128 v[124:127], v17 offset:20480\n"
      "ds_read_b128 v[128:131], v25 offset:8192\n"
      "ds_read_b128 v[132:135], v25 offset:28672\n"
      "ds_read_b128 v[136:139], v25 offset:49152\n"
      "ds_read_b128 v[140:143], v16 offset:8192\n"
      "ds_read_b128 v[144:147], v16 offset:28672\n"
      "ds_read_b128 v[148:151], v16 offset:49152\n"
      "ds_read_b128 v[152:155], v17 offset:8192\n"
      "ds_read_b128 v[156:159], v17 offset:28672\n"
      "s_waitcnt lgkmcnt(8)\n"
      "v_pk_add_f32 v[96:97], v[96:97], v[100:101]\n"
      "v_pk_add_f32 v[98:99], v[98:99], v[102:103]\n"
      "v_pk_add_f32 v[104:105], v[104:105], v[108:109]\n"
      "v_pk_add_f32 v[106:107], v[106:107], v[110:111]\n"
      "v_pk_add_f32 v[112:113], v[112:113], v[116:117]\n"
      "v_pk_add_f32 v[114:115], v[114:115], v[118:119]\n"
      "v_pk_add_f32 v[120:121], v[120:121], v[124:125]\n"
      "v_pk_add_f32 v[122:123], v[122:123], v[126:127]\n"
      "v_pk_add_f32 v[96:97], v[96:97], v[104:105]\n"
      "v_pk_add_f32 v[98:99], v[98:99], v[106:107]\n"
      "v_pk_add_f32 v[112:113], v[112:113], v[120:121]\n"
      "v_pk_add_f32 v[114:115], v[114:115], v[122:123]\n"
      "v_pk_add_f32 v[96:97], v[96:97], v[112:113]\n"
      "v_pk_add_f32 v[98:99], v[98:99], v[114:115]\n"
      "v_cvt_pk_bf16_f32 v96, v96, v97\n"
      "v_cvt_pk_bf16_f32 v97, v98, v99\n"
      "buffer_store_dwordx2 v[96:97], v26, s[56:59], s49 offen\n"
      "s_waitcnt lgkmcnt(0)\n"
      "v_pk_add_f32 v[128:129], v[128:129], v[132:133]\n"
      "v_pk_add_f32 v[130:131], v[130:131], v[134:135]\n"
      "v_pk_add_f32 v[136:137], v[136:137], v[140:141]\n"
      "v_pk_add_f32 v[138:139], v[138:139], v[142:143]\n"
      "v_pk_add_f32 v[144:145], v[144:145], v[148:149]\n"
      "v_pk_add_f32 v[146:147], v[146:147], v[150:151]\n"
      "v_pk_add_f32 v[152:153], v[152:153], v[156:157]\n"
      "v_pk_add_f32 v[154:155], v[154:155], v[158:159]\n"
      "v_pk_add_f32 v[128:129], v[128:129], v[136:137]\n"
      "v_pk_add_f32 v[130:131], v[130:131], v[138:139]\n"
      "v_pk_add_f32 v[144:145], v[144:145], v[152:153]\n"
      "v_pk_add_f32 v[146:147], v[146:147], v[154:155]\n"
      "v_pk_add_f32 v[128:129], v[128:129], v[144:145]\n"
      "v_pk_add_f32 v[130:131], v[130:131], v[146:147]\n"
      "v_cvt_pk_bf16_f32 v128, v128, v129\n"
      "v_cvt_pk_bf16_f32 v129, v130, v131\n"
      "buffer_store_dwordx2 v[128:129], v26, s[56:59], s50 offen\n"
      "s_cmp_ge_u32 s64, 4\n"
      "s_cbranch_scc1 L_nor2\n"
      "ds_read_b128 v[160:163], v25 offset:16384\n"
      "ds_read_b128 v[164:167], v25 offset:36864\n"
      "ds_read_b128 v[168:171], v25 offset:57344\n"
      "ds_read_b128 v[172:175], v16 offset:16384\n"
      "ds_read_b128 v[176:179], v16 offset:36864\n"
      "ds_read_b128 v[180:183], v16 offset:57344\n"
      "ds_read_b128 v[184:187], v17 offset:16384\n"
      "ds_read_b128 v[188:191], v17 offset:36864\n"
      "s_waitcnt lgkmcnt(0)\n"
      "v_pk_add_f32 v[160:161], v[160:161], v[164:165]\n"
      "v_pk_add_f32 v[162:163], v[162:163], v[166:167]\n"
      "v_pk_add_f32 v[168:169], v[168:169], v[172:173]\n"
      "v_pk_add_f32 v[170:171], v[170:171], v[174:175]\n"
      "v_pk_add_f32 v[176:177], v[176:177], v[180:181]\n"
      "v_pk_add_f32 v[178:179], v[178:179], v[182:183]\n"
      "v_pk_add_f32 v[184:185], v[184:185], v[188:189]\n"
      "v_pk_add_f32 v[186:187], v[186:187], v[190:191]\n"
      "v_pk_add_f32 v[160:161], v[160:161], v[168:169]\n"
      "v_pk_add_f32 v[162:163], v[162:163], v[170:171]\n"
      "v_pk_add_f32 v[176:177], v[176:177], v[184:185]\n"
      "v_pk_add_f32 v[178:179], v[178:179], v[186:187]\n"
      "v_pk_add_f32 v[160:161], v[160:161], v[176:177]\n"
      "v_pk_add_f32 v[162:163], v[162:163], v[178:179]\n"
      "v_cvt_pk_bf16_f32 v160, v160, v161\n"
      "v_cvt_pk_bf16_f32 v161, v162, v163\n"
      "buffer_store_dwordx2 v[160:161], v26, s[56:59], s51 offen\n"
      "s_or_b32 s73, s65, s66\n"
      "s_or_b32 s73, s73, s64\n"
      "s_cmp_lg_u32 s73, 0\n"
      "s_cbranch_scc1 L_nor2\n"
      "s_mov_b64 exec, 1\n"
      "buffer_store_dword v2, off, s[60:63], s73 sc1\n"
      "s_mov_b64 exec, -1\n"
      "L_nor2:\n"
      "L_end:\n"
      "s_endpgm\n"
      "L_pre:\n"
      "s_mov_b32 s32, s10\n"
      "s_and_b32 s33, s11, 0xffff\n"
      "s_mov_b32 s34, 2457600\n"
      "s_mov_b32 s35, 0x20000\n"
      "s_mov_b32 s36, s12\n"
      "s_and_b32 s37, s13, 0xffff\n"
      "s_mov_b32 s38, 25600\n"
      "s_mov_b32 s39, 0x20000\n"
      "s_cmp_eq_u32 s66, 30\n"
      "s_cselect_b32 s73, 4, s70\n"
      "s_mul_i32 s11, s65, 5\n"
      "s_add_u32 s11, s11, s73\n"
      "s_and_b32 s75, s11, 1\n"
      "s_lshl_b32 s55, s75, 3\n"
      "s_add_u32 s55, s55, s64\n"
      "s_lshl_b32 s55, s55, 1\n"
      "v_lshlrev_b32 v69, 3, v0\n"
      "s_mul_i32 s73, s55, 800\n"
      "buffer_load_dwordx2 v[164:165], v69, s[36:39], s73 offen\n"
      "buffer_load_dwordx2 v[168:169], v69, s[36:39], s73 offen offset:800\n"
      "s_mov_b32 exec_hi, 15\n"
      "buffer_load_dwordx2 v[166:167], v69, s[36:39], s73 offen offset:512\n"
      "buffer_load_dwordx2 v[170:171], v69, s[36:39], s73 offen offset:1312\n"
      "s_lshr_b32 s73, s11, 2\n"
      "s_lshl_b32 s73, s73, 6\n"
      "s_and_b32 s74, s11, 2\n"
      "s_lshl_b32 s74, s74, 3\n"
      "s_add_u32 s77, s73, s74\n"
      "s_and_b32 s73, s64, 1\n"
      "s_lshl_b32 s73, s73, 5\n"
      "s_lshr_b32 s74, s64, 1\n"
      "s_lshl_b32 s74, s74, 2\n"
      "s_add_u32 s73, s73, s74\n"
      "s_add_u32 s73, s73, s77\n"
      "s_mul_i32 s73, s73, 3840\n"
      "s_mul_i32 s75, s75, 640\n"
      "s_add_u32 s73, s73, s75\n"
      "s_mul_i32 s74, s64, 2624\n"
      "s_mov_b32 exec_hi, 255\n"
      "s_add_u32 m0, s74, 0\n"
      "s_add_u32 s97, s73, 0\n"
      "buffer_load_dwordx4 v5, s[32:35], s97 offen lds\n"
      "s_add_u32 m0, s74, 20992\n"
      "s_add_u32 s97, s73, 1280\n"
      "buffer_load_dwordx4 v5, s[32:35], s97 offen lds\n"
      "s_add_u32 m0, s74, 41984\n"
      "s_add_u32 s97, s73, 2560\n"
      "buffer_load_dwordx4 v5, s[32:35], s97 offen lds\n"
      "s_add_u32 m0, s74, 656\n"
      "s_add_u32 s97, s73, 3840\n"
      "buffer_load_dwordx4 v5, s[32:35], s97 offen lds\n"
      "s_add_u32 m0, s74, 21648\n"
      "s_add_u32 s97, s73, 5120\n"
      "buffer_load_dwordx4 v5, s[32:35], s97 offen lds\n"
      "s_add_u32 m0, s74, 42640\n"
      "s_add_u32 s97, s73, 6400\n"
      "buffer_load_dwordx4 v5, s[32:35], s97 offen lds\n"
      "s_add_u32 m0, s74, 1312\n"
      "s_add_u32 s97, s73, 7680\n"
      "buffer_load_dwordx4 v5, s[32:35], s97 offen lds\n"
      "s_add_u32 m0, s74, 22304\n"
      "s_add_u32 s97, s73, 8960\n"
      "buffer_load_dwordx4 v5, s[32:35], s97 offen lds\n"
      "s_add_u32 m0, s74, 43296\n"
      "s_add_u32 s97, s73, 10240\n"
      "buffer_load_dwordx4 v5, s[32:35], s97 offen lds\n"
      "s_add_u32 m0, s74, 1968\n"
      "s_add_u32 s97, s73, 11520\n"
      "buffer_load_dwordx4 v5, s[32:35], s97 offen lds\n"
      "s_add_u32 m0, s74, 22960\n"
      "s_add_u32 s97, s73, 12800\n"
      "buffer_load_dwordx4 v5, s[32:35], s97 offen lds\n"
      "s_add_u32 m0, s74, 43952\n"
      "s_add_u32 s97, s73, 14080\n"
      "buffer_load_dwordx4 v5, s[32:35], s97 offen lds\n"
      "s_mov_b32 exec_hi, -1\n"
      "s_getpc_b64 s[48:49]\n"
      "L_pc2:\n"
      "s_and_b32 s49, s49, 0xffff\n"
      "s_mov_b32 s50, L_end2-L_pc2\n"
      "s_mov_b32 s51, 0x20000\n"
      "s_lshl_b32 s73, s64, 10\n"
      "v_lshlrev_b32 v254, 7, v0\n"
      "v_add_u32 v254, s73, v254\n"
      "s_mov_b64 exec, 0xff\n"
      "buffer_load_dword v252, v254, s[48:51], 0 offen\n"
      "s_mov_b64 exec, -1\n"
      "s_mov_b32 s56, s20\n"
      "s_and_b32 s57, s21, 0xffff\n"
      "s_mov_b32 s58, 1280\n"
      "s_mov_b32 s59, 0x20000\n"
      "v_mov_b32 v160, 0\n"
      "v_mov_b32 v161, 0\n"
      "v_mov_b32 v162, 0\n"
      "v_mov_b32 v163, 0\n"
      "v_add_u32 v80, s27, v5\n"
      "ds_write_b128 v80, v[160:163] offset:0\n"
      "ds_write_b128 v80, v[160:163] offset:1024\n"
      "ds_write_b128 v80, v[160:163] offset:2048\n"
      "ds_write_b128 v80, v[160:163] offset:3072\n"
      "ds_write_b128 v80, v[160:163] offset:4096\n"
      "ds_write_b128 v80, v[160:163] offset:5120\n"
      "ds_write_b128 v80, v[160:163] offset:6144\n"
      "ds_write_b128 v80, v[160:163] offset:7168\n"
      "ds_write_b128 v80, v[160:163] offset:8192\n"
      "ds_write_b128 v80, v[160:163] offset:9216\n"
      "v_bfe_u32 v65, v0, 2, 1\n"
      "v_lshl_add_u32 v66, v65, 5, v25\n"
      "v_bfe_u32 v65, v0, 3, 2\n"
      "v_lshl_add_u32 v66, v65, 2, v66\n"
      "v_add_lshl_u32 v70, v66, s77, 1\n"
      "v_cmp_le_u32 vcc, 16, v0\n"
      "s_nop 1\n"
      "v_cndmask_b32 v64, 0, 4, vcc\n"
      "v_add_u32 v64, v64, v0\n"
      "v_add_u32 v64, 48, v64\n"
      "v_cmp_le_u32 vcc, 10, v0\n"
      "s_nop 1\n"
      "v_cndmask_b32 v65, 0, 1, vcc\n"
      "v_mul_u32_u24 v66, 10, v65\n"
      "v_sub_u32 v66, v0, v66\n"
      "v_lshrrev_b32 v67, 2, v66\n"
      "v_lshlrev_b32 v67, 3, v67\n"
      "v_lshl_add_u32 v67, v65, 2, v67\n"
      "v_and_b32 v66, 3, v66\n"
      "v_add3_u32 v67, v67, v66, 40\n"
      "v_cmp_le_u32 vcc, 20, v0\n"
      "v_mov_b32 v66, 72\n"
      "s_nop 0\n"
      "v_cndmask_b32 v64, v64, v66, vcc\n"
      "v_lshlrev_b32 v66, 2, v0\n"
      "v_subrev_u32 v66, 22, v66\n"
      "v_cndmask_b32 v67, v67, v66, vcc\n"
      "v_lshlrev_b32 v64, 7, v64\n"
      "v_lshl_add_u32 v64, v67, 1, v64\n"
      "v_add_u32 v64, s27, v64\n"
      "v_mov_b32 v65, 0x3f80\n"
      "s_mov_b64 exec, 0x3fffff\n"
      "ds_write_b16 v64, v65\n"
      "s_mov_b64 exec, -1\n"
      "v_add_u32 v64, 0, v0\n"
      "v_mul_u32_u24 v65, 205, v64\n"
      "v_lshrrev_b32 v65, 10, v65\n"
      "v_mul_u32_u24 v66, 5, v65\n"
      "v_sub_u32 v66, v64, v66\n"
      "v_cmp_le_u32 vcc, 16, v65\n"
      "s_nop 1\n"
      "v_cndmask_b32 v67, 0, 4, vcc\n"
      "v_add_u32 v65, v65, v67\n"
      "v_lshlrev_b32 v65, 7, v65\n"
      "v_lshl_add_u32 v65, v66, 4, v65\n"
      "v_add_u32 v68, s27, v65\n"
      "v_add_u32 v64, 64, v0\n"
      "v_mul_u32_u24 v65, 205, v64\n"
      "v_lshrrev_b32 v65, 10, v65\n"
      "v_mul_u32_u24 v66, 5, v65\n"
      "v_sub_u32 v66, v64, v66\n"
      "v_cmp_le_u32 vcc, 16, v65\n"
      "s_nop 1\n"
      "v_cndmask_b32 v67, 0, 4, vcc\n"
      "v_add_u32 v65, v65, v67\n"
      "v_lshlrev_b32 v65, 7, v65\n"
      "v_lshl_add_u32 v65, v66, 4, v65\n"
      "v_add_u32 v69, s27, v65\n"
      "v_lshl_add_u32 v64, v27, 3, v24\n"
      "v_lshlrev_b32 v64, 7, v64\n"
      "v_lshl_add_u32 v64, v26, 5, v64\n"
      "v_lshl_add_u32 v64, v25, 3, v64\n"
      "v_add_u32 v64, s27, v64\n"
      "s_waitcnt lgkmcnt(0)\n"
      "s_mov_b32 exec_lo, 0\n"
      "buffer_load_ushort v160, v70, s[56:59], 0 offen\n"
      "s_mov_b32 exec_lo, -1\n"
      "s_waitcnt vmcnt(14)\n"
      "ds_write_b64 v68, v[164:165]\n"
      "ds_write_b64 v68, v[164:165] offset:3080\n"
      "s_mov_b32 exec_hi, 15\n"
      "ds_write_b64 v69, v[166:167]\n"
      "ds_write_b64 v69, v[166:167] offset:3080\n"
      "s_mov_b32 exec_hi, -1\n"
      "ds_read_b64_tr_b16 v[172:173], v64 offset:64\n"
      "ds_read_b64_tr_b16 v[174:175], v64 offset:576\n"
      "ds_read_b64_tr_b16 v[176:177], v64 offset:0\n"
      "ds_read_b64_tr_b16 v[178:179], v64 offset:512\n"
      "ds_read_b64_tr_b16 v[180:181], v64 offset:4160\n"
      "ds_read_b64_tr_b16 v[182:183], v64 offset:4672\n"
      "ds_read_b64_tr_b16 v[184:185], v64 offset:4096\n"
      "ds_read_b64_tr_b16 v[186:187], v64 offset:4608\n"
      "ds_read_b64_tr_b16 v[188:189], v64 offset:2112\n"
      "ds_read_b64_tr_b16 v[190:191], v64 offset:2624\n"
      "ds_read_b64_tr_b16 v[192:193], v64 offset:2048\n"
      "ds_read_b64_tr_b16 v[194:195], v64 offset:2560\n"
      "ds_read_b64_tr_b16 v[196:197], v64 offset:6208\n"
      "ds_read_b64_tr_b16 v[198:199], v64 offset:6720\n"
      "ds_read_b64_tr_b16 v[200:201], v64 offset:8256\n"
      "ds_read_b64_tr_b16 v[202:203], v64 offset:8768\n"
      "ds_write_b64 v68, v[168:169]\n"
      "ds_write_b64 v68, v[168:169] offset:3080\n"
      "s_mov_b32 exec_hi, 15\n"
      "ds_write_b64 v69, v[170:171]\n"
      "ds_write_b64 v69, v[170:171] offset:3080\n"
      "s_mov_b32 exec_hi, -1\n"
      "ds_read_b64_tr_b16 v[116:117], v64 offset:64\n"
      "ds_read_b64_tr_b16 v[118:119], v64 offset:576\n"
      "ds_read_b64_tr_b16 v[120:121], v64 offset:0\n"
      "ds_read_b64_tr_b16 v[122:123], v64 offset:512\n"
      "ds_read_b64_tr_b16 v[124:125], v64 offset:4160\n"
      "ds_read_b64_tr_b16 v[126:127], v64 offset:4672\n"
      "ds_read_b64_tr_b16 v[128:129], v64 offset:4096\n"
      "ds_read_b64_tr_b16 v[130:131], v64 offset:4608\n"
      "ds_read_b64_tr_b16 v[132:133], v64 offset:2112\n"
      "ds_read_b64_tr_b16 v[134:135], v64 offset:2624\n"
      "ds_read_b64_tr_b16 v[136:137], v64 offset:2048\n"
      "ds_read_b64_tr_b16 v[138:139], v64 offset:2560\n"
      "v_and_b32 v70, 31, v0\n"
      "v_mul_u32_u24 v70, 656, v70\n"
      "s_mul_i32 s73, s64, 80\n"
      "v_add_u32 v70, s73, v70\n"
      "v_lshl_add_u32 v65, v27, 4, v70\n"
      "v_mul_u32_u24 v66, 20968, v27\n"
      "v_add_u32 v66, v66, v70\n"
      "v_lshl_add_u32 v67, v27, 3, v70\n"
      "s_mul_i32 s76, s55, 81920\n"
      "s_lshr_b32 s77, s11, 1\n"
      "s_lshl_b32 s77, s77, 12\n"
      "s_add_u32 s76, s76, s77\n"
      "s_add_u32 s19, s76, 1048576\n"
      "s_mov_b32 s60, s24\n"
      "s_and_b32 s61, s25, 0xffff\n"
      "s_mov_b32 s62, 23068672\n"
      "s_mov_b32 s63, 0x20000\n"
      "s_mov_b32 s56, s22\n"
      "s_and_b32 s57, s23, 0xffff\n"
      "s_mov_b32 s58, 2621440\n"
      "s_mov_b32 s59, 0x20000\n"
      "s_lshl_b32 s34, s69, 13\n"
      "s_mul_i32 s35, s66, 81920\n"
      "s_add_u32 s35, s35, 1048576\n"
      "s_add_u32 s35, s35, s53\n"
      "v_and_b32 v22, 31, v0\n"
      "v_lshlrev_b32 v22, 7, v22\n"
      "v_add_u32 v22, s27, v22\n"
      "v_bfe_u32 v20, v0, 1, 3\n"
      "v_lshlrev_b32 v21, 2, v27\n"
      "v_xor_b32 v20, v20, v21\n"
      "v_lshl_add_u32 v12, v20, 4, v22\n"
      "v_xor_b32 v13, 16, v12\n"
      "v_xor_b32 v14, 32, v12\n"
      "v_xor_b32 v15, 48, v12\n"
      "v_lshl_add_u32 v22, v27, 5, v24\n"
      "v_lshlrev_b32 v22, 7, v22\n"
      "v_lshl_add_u32 v22, v26, 5, v22\n"
      "v_lshl_add_u32 v22, v25, 3, v22\n"
      "v_lshrrev_b32 v23, 1, v24\n"
      "v_lshl_add_u32 v3, v23, 6, v22\n"
      "v_xor_b32 v4, 64, v3\n"
      "s_waitcnt lgkmcnt(0)\n"
      "s_add_u32 s26, s26, 1\n"
      "v_mov_b32 v2, s26\n"
      "s_waitcnt vmcnt(0)\n"
      "s_barrier\n"
      "v_mov_b32 v112, v160\n"
      "v_mov_b32 v113, v161\n"
      "v_mov_b32 v114, v162\n"
      "v_mov_b32 v115, v163\n"
      "s_mov_b32 exec_hi, 0\n"
      "ds_read_b64 v[160:161], v70 offset:42008\n"
      "ds_read_b64 v[162:163], v70 offset:42016\n"
      "s_mov_b32 exec_hi, -1\n"
      "ds_read_b64 v[144:145], v65 offset:0\n"
      "ds_read_b64 v[146:147], v65 offset:8\n"
      "ds_read_b64 v[148:149], v66 offset:24\n"
      "ds_read_b64 v[150:151], v66 offset:32\n"
      "ds_read_b64 v[152:153], v67 offset:21008\n"
      "ds_read_b64 v[154:155], v67 offset:21016\n"
      "ds_read_b64 v[156:157], v65 offset:41984\n"
      "ds_read_b64 v[158:159], v65 offset:41992\n"
      "s_mov_b32 exec_hi, 0\n"
      "ds_read_b64 v[112:113], v70 offset:42048\n"
      "ds_read_b64 v[114:115], v70 offset:42056\n"
      "s_mov_b32 exec_hi, -1\n"
      "ds_read_b64 v[96:97], v65 offset:40\n"
      "ds_read_b64 v[98:99], v65 offset:48\n"
      "ds_read_b64 v[100:101], v66 offset:64\n"
      "ds_read_b64 v[102:103], v66 offset:72\n"
      "ds_read_b64 v[104:105], v67 offset:21048\n"
      "ds_read_b64 v[106:107], v67 offset:21056\n"
      "ds_read_b64 v[108:109], v65 offset:42024\n"
      "ds_read_b64 v[110:111], v65 offset:42032\n"
      "s_add_u32 m0, s27, 0\n"
      "s_setprio 0\n"
      "buffer_load_dwordx4 v8, s[28:31], s72 offen lds\n"
      "s_setprio 1\n"
      "s_setprio 0\n"
      "buffer_load_dwordx4 v9, s[28:31], s72 offen offset:1024 lds\n"
      "s_setprio 1\n"
      "s_setprio 0\n"
      "buffer_load_dwordx4 v10, s[28:31], s72 offen offset:2048 lds\n"
      "s_setprio 1\n"
      "s_add_u32 m0, s27, 0\n"
      "s_setprio 0\n"
      "buffer_load_dwordx4 v11, s[28:31], s72 offen offset:3072 lds\n"
      "s_setprio 1\n"
      "s_add_u32 s72, s72, 128\n"
      "s_cmp_eq_u32 s72, 1280\n"
      "s_cselect_b32 s72, 0, s72\n"
      "s_waitcnt lgkmcnt(0)\n"
      "v_mfma_f32_32x32x16_bf16 v[240:255], v[144:147], v[172:175], 0\n"
      "v_mfma_f32_32x32x16_bf16 v[224:239], v[144:147], v[176:179], 0\n"
      "v_mfma_f32_32x32x16_bf16 v[240:255], v[152:155], v[180:183], v[240:255]\n"
      "v_mfma_f32_32x32x16_bf16 v[224:239], v[152:155], v[184:187], v[224:239]\n"
      "v_mfma_f32_32x32x16_bf16 v[240:255], v[148:151], v[188:191], v[240:255]\n"
      "v_mfma_f32_32x32x16_bf16 v[224:239], v[148:151], v[192:195], v[224:239]\n"
      "v_mfma_f32_32x32x16_bf16 v[240:255], v[156:159], v[196:199], v[240:255]\n"
      "v_mfma_f32_32x32x16_bf16 v[240:255], v[160:163], v[200:203], v[240:255]\n"
      "v_mfma_f32_32x32x16_bf16 v[48:63], v[96:99], v[116:119], 0\n"
      "v_mfma_f32_32x32x16_bf16 v[32:47], v[96:99], v[120:123], 0\n"
      "v_mfma_f32_32x32x16_bf16 v[48:63], v[104:107], v[124:127], v[48:63]\n"
      "v_mfma_f32_32x32x16_bf16 v[32:47], v[104:107], v[128:131], v[32:47]\n"
      "v_mfma_f32_32x32x16_bf16 v[48:63], v[100:103], v[132:135], v[48:63]\n"
      "v_mfma_f32_32x32x16_bf16 v[32:47], v[100:103], v[136:139], v[32:47]\n"
      "v_mfma_f32_32x32x16_bf16 v[48:63], v[108:111], v[196:199], v[48:63]\n"
      "v_mfma_f32_32x32x16_bf16 v[48:63], v[112:115], v[200:203], v[48:63]\n"
      "s_nop 1\n"
      "v_cvt_pk_bf16_f32 v204, v224, v225\n"
      "v_cvt_pk_bf16_f32 v205, v226, v227\n"
      "v_cvt_pk_bf16_f32 v206, v228, v229\n"
      "v_cvt_pk_bf16_f32 v207, v230, v231\n"
      "buffer_store_dwordx4 v[204:207], v5, s[60:63], s19 offen offset:0 sc1\n"
      "v_cvt_pk_bf16_f32 v208, v232, v233\n"
      "v_cvt_pk_bf16_f32 v209, v234, v235\n"
      "v_cvt_pk_bf16_f32 v210, v236, v237\n"
      "v_cvt_pk_bf16_f32 v211, v238, v239\n"
      "buffer_store_dwordx4 v[208:211], v5, s[60:63], s19 offen offset:2048 sc1\n"
      "v_cvt_pk_bf16_f32 v212, v240, v241\n"
      "v_cvt_pk_bf16_f32 v213, v242, v243\n"
      "v_cvt_pk_bf16_f32 v214, v244, v245\n"
      "v_cvt_pk_bf16_f32 v215, v246, v247\n"
      "buffer_store_dwordx4 v[212:215], v5, s[60:63], s19 offen offset:1024 sc1\n"
      "v_cvt_pk_bf16_f32 v216, v248, v249\n"
      "v_cvt_pk_bf16_f32 v217, v250, v251\n"
      "v_cvt_pk_bf16_f32 v218, v252, v253\n"
      "v_cvt_pk_bf16_f32 v219, v254, v255\n"
      "buffer_store_dwordx4 v[216:219], v5, s[60:63], s19 offen offset:3072 sc1\n"
      "s_add_u32 s19, s19, 81920\n"
      "v_cvt_pk_bf16_f32 v144, v32, v33\n"
      "v_cvt_pk_bf16_f32 v145, v34, v35\n"
      "v_cvt_pk_bf16_f32 v146, v36, v37\n"
      "v_cvt_pk_bf16_f32 v147, v38, v39\n"
      "buffer_store_dwordx4 v[144:147], v5, s[60:63], s19 offen offset:0 sc1\n"
      "v_cvt_pk_bf16_f32 v148, v40, v41\n"
      "v_cvt_pk_bf16_f32 v149, v42, v43\n"
      "v_cvt_pk_bf16_f32 v150, v44, v45\n"
      "v_cvt_pk_bf16_f32 v151, v46, v47\n"
      "buffer_store_dwordx4 v[148:151], v5, s[60:63], s19 offen offset:2048 sc1\n"
      "v_cvt_pk_bf16_f32 v152, v48, v49\n"
      "v_cvt_pk_bf16_f32 v153, v50, v51\n"
      "v_cvt_pk_bf16_f32 v154, v52, v53\n"
      "v_cvt_pk_bf16_f32 v155, v54, v55\n"
      "buffer_store_dwordx4 v[152:155], v5, s[60:63], s19 offen offset:1024 sc1\n"
      "v_cvt_pk_bf16_f32 v156, v56, v57\n"
      "v_cvt_pk_bf16_f32 v157, v58, v59\n"
      "v_cvt_pk_bf16_f32 v158, v60, v61\n"
      "v_cvt_pk_bf16_f32 v159, v62, v63\n"
      "buffer_store_dwordx4 v[156:159], v5, s[60:63], s19 offen offset:3072 sc1\n"
      "s_add_u32 s19, s19, 81920\n"
      "s_mov_b32 s5, s55\n"
      "s_mov_b64 s[36:37], s[60:61]\n"
      "s_mov_b32 s39, 0x20000\n"
      "s_cmp_ge_u32 s64, 4\n"
      "s_cselect_b32 s38, 23068672, 0\n"
      "s_lshl_b32 s73, s65, 2\n"
      "s_lshr_b32 s74, s64, 1\n"
      "s_add_u32 s73, s73, s74\n"
      "s_and_b32 s76, s64, 1\n"
      "s_mul_i32 s74, s73, 54\n"
      "s_lshl_b32 s75, s67, 1\n"
      "s_add_u32 s74, s74, s75\n"
      "s_lshl_b32 s74, s74, 12\n"
      "s_lshl_b32 s77, s76, 11\n"
      "s_add_u32 s74, s74, s77\n"
      "s_add_u32 s20, s74, 8388608\n"
      "s_lshl_b32 s21, s73, 8\n"
      "s_add_u32 s75, s75, s76\n"
      "s_lshl_b32 s75, s75, 2\n"
      "s_add_u32 s21, s21, s75\n"
      "s_add_u32 s21, s21, 12288\n"
      "s_mul_i32 s74, s73, 81920\n"
      "s_add_u32 s74, s74, s77\n"
      "s_and_b32 s75, s66, 1\n"
      "s_lshl_b32 s75, s75, 9\n"
      "s_add_u32 s74, s74, s75\n"
      "s_add_u32 s74, s74, 16777216\n"
      "s_lshr_b32 s75, s66, 1\n"
      "s_lshl_b32 s75, s75, 12\n"
      "s_add_u32 s24, s75, s74\n"
      "s_lshl_b32 s77, s70, 12\n"
      "s_add_u32 s74, s74, s77\n"
      "s_lshr_b32 s77, s71, 1\n"
      "s_lshl_b32 s77, s77, 7\n"
      "s_add_u32 s74, s74, s77\n"
      "s_add_u32 s25, s74, 65536\n"
      "s_lshl_b32 s40, s73, 8\n"
      "s_lshl_b32 s77, s66, 1\n"
      "s_add_u32 s77, s77, s76\n"
      "s_lshl_b32 s77, s77, 2\n"
      "s_add_u32 s40, s40, s77\n"
      "s_add_u32 s40, s40, 20480\n"
      "s_add_u32 s73, s65, s71\n"
      "s_and_b32 s73, s73, 0\n"
      "s_lshl_b32 s73, s73, 2\n"
      "s_xor_b32 s54, s64, s73\n"
      "s_lshl_b32 s23, s66, 16\n"
      "s_add_u32 s23, s23, 20971520\n"
      "s_lshl_b32 s73, s65, 13\n"
      "s_add_u32 s22, s23, s73\n"
      "s_mul_i32 s73, s71, 74\n"
      "s_add_u32 s74, s73, s54\n"
      "s_mul_i32 s75, s74, 5120\n"
      "s_add_u32 s79, s75, 4194304\n"
      "s_add_u32 s42, s75, 4532224\n"
      "s_add_u32 s74, s74, s54\n"
      "s_mul_i32 s74, s74, 5120\n"
      "s_add_u32 s41, s74, 4470784\n"
      "s_lshl_b32 s73, s65, 2\n"
      "s_add_u32 s73, s73, s70\n"
      "s_mul_i32 s78, s73, 54\n"
      "s_add_u32 s78, s78, s54\n"
      "s_lshl_b32 s78, s78, 12\n"
      "s_add_u32 s78, s78, 8388608\n"
      "s_mul_i32 s77, s73, 81920\n"
      "s_add_u32 s77, s77, 16777216\n"
      "s_lshl_b32 s74, s54, 13\n"
      "s_add_u32 s43, s74, s77\n"
      "s_lshl_b32 s74, s54, 12\n"
      "s_add_u32 s74, s74, s77\n"
      "s_add_u32 s48, s74, 49152\n"
      "s_mul_i32 s73, s73, 81920\n"
      "s_mul_i32 s74, s71, 160\n"
      "s_add_u32 s73, s73, s74\n"
      "s_add_u32 s74, s64, 0\n"
      "s_mul_i32 s75, s74, 52\n"
      "s_lshr_b32 s75, s75, 8\n"
      "s_mul_i32 s75, s75, 20320\n"
      "s_lshl_b32 s74, s74, 5\n"
      "s_add_u32 s74, s74, s75\n"
      "s_add_u32 s49, s74, s73\n"
      "s_add_u32 s74, s64, 8\n"
      "s_mul_i32 s75, s74, 52\n"
      "s_lshr_b32 s75, s75, 8\n"
      "s_mul_i32 s75, s75, 20320\n"
      "s_lshl_b32 s74, s74, 5\n"
      "s_add_u32 s74, s74, s75\n"
      "s_add_u32 s50, s74, s73\n"
      "s_add_u32 s74, s64, 16\n"
      "s_mul_i32 s75, s74, 52\n"
      "s_lshr_b32 s75, s75, 8\n"
      "s_mul_i32 s75, s75, 20320\n"
      "s_lshl_b32 s74, s74, 5\n"
      "s_add_u32 s74, s74, s75\n"
      "s_add_u32 s51, s74, s73\n"
      "v_and_b32 v6, 15, v0\n"
      "v_bfe_u32 v7, v0, 4, 1\n"
      "v_lshlrev_b32 v7, 10, v7\n"
      "v_lshl_add_u32 v23, v6, 3, v7\n"
      "s_lshl_b32 s73, s70, 6\n"
      "v_add_lshl_u32 v6, v6, s73, 2\n"
      "v_add_u32 v30, 38720, v6\n"
      "v_and_b32 v6, 31, v0\n"
      "s_lshl_b32 s73, s64, 5\n"
      "v_add_lshl_u32 v7, v6, s73, 2\n"
      "v_add_u32 v27, 38720, v7\n"
      "v_mul_u32_u24 v6, 144, v6\n"
      "s_mul_i32 s73, s64, 4608\n"
      "v_add_u32 v16, s73, v6\n"
      "v_lshrrev_b32 v31, 5, v0\n"
      "v_mul_u32_u24 v31, 24, v31\n"
      "v_add_u32 v17, v31, v16\n"
      "v_lshrrev_b32 v6, 4, v0\n"
      "v_bfe_u32 v7, v0, 2, 2\n"
      "v_lshl_add_u32 v7, v6, 3, v7\n"
      "v_mul_u32_u24 v7, 144, v7\n"
      "v_and_b32 v31, 3, v0\n"
      "v_lshl_add_u32 v18, v31, 3, v7\n"
      "s_lshl_b32 s73, s64, 2\n"
      "v_add_u32 v6, s73, v6\n"
      "v_and_b32 v7, 15, v0\n"
      "v_cmp_gt_u32 s[16:17], 10, v6\n"
      "v_cmp_gt_u32 vcc, 6, v7\n"
      "s_and_b64 s[18:19], vcc, s[16:17]\n"
      "v_mul_u32_u24 v6, 22, v6\n"
      "v_add_lshl_u32 v19, v6, v7, 4\n"
      "s_lshl_b32 s73, s64, 6\n"
      "v_add_u32 v31, s73, v0\n"
      "v_min_u32 v31, 219, v31\n"
      "v_lshlrev_b32 v29, 4, v31\n"
      "v_mul_u32_u24 v6, 2979, v31\n"
      "v_lshrrev_b32 v6, 16, v6\n"
      "v_mul_u32_u24 v7, 22, v6\n"
      "v_sub_u32 v7, v31, v7\n"
      "v_mul_u32_u24 v7, 80, v7\n"
      "v_lshl_add_u32 v6, v6, 3, v7\n"
      "v_add_u32 v21, 36864, v6\n"
      "v_bfe_u32 v6, v0, 3, 2\n"
      "v_bfe_u32 v7, v0, 2, 1\n"
      "v_and_b32 v31, 3, v0\n"
      "v_lshlrev_b32 v7, 3, v7\n"
      "v_lshl_add_u32 v7, v6, 2, v7\n"
      "v_cmp_eq_u32 vcc, 2, v6\n"
      "s_nop 1\n"
      "v_cndmask_b32 v7, v7, 16, vcc\n"
      "v_add_u32 v7, v7, v31\n"
      "v_cmp_le_u32 vcc, 10, v7\n"
      "s_nop 1\n"
      "v_cndmask_b32 v31, 0, 2, vcc\n"
      "v_add_u32 v7, v7, v31\n"
      "v_cmp_eq_u32 vcc, 3, v6\n"
      "s_nop 1\n"
      "v_cndmask_b32 v7, v7, 11, vcc\n"
      "v_cmp_le_u32 vcc, 32, v0\n"
      "s_nop 1\n"
      "v_cndmask_b32 v7, v7, 22, vcc\n"
      "v_mul_u32_u24 v7, 80, v7\n"
      "v_add_u32 v22, 36864, v7\n"
      "v_lshrrev_b32 v6, 4, v0\n"
      "v_lshlrev_b32 v6, 6, v6\n"
      "v_xor_b32 v6, v6, v5\n"
      "s_mul_i32 s73, s64, 20480\n"
      "v_add_u32 v24, s73, v6\n"
      "v_lshrrev_b32 v7, 2, v0\n"
      "v_and_b32 v31, 3, v0\n"
      "v_lshlrev_b32 v6, 2, v31\n"
      "v_xor_b32 v6, v6, v7\n"
      "v_lshl_add_u32 v6, v31, 4, v6\n"
      "v_lshlrev_b32 v6, 4, v6\n"
      "v_add_u32 v25, s53, v6\n"
      "v_mul_u32_u24 v7, 1280, v7\n"
      "v_lshl_add_u32 v26, v31, 3, v7\n"
      "v_add_u32 v20, s27, v5\n"
      "s_waitcnt vmcnt(0)\n"
      "s_lshl_b32 s73, s5, 7\n"
      "s_lshr_b32 s74, s11, 1\n"
      "s_lshl_b32 s74, s74, 2\n"
      "s_add_u32 s73, s73, s74\n"
      "s_add_u32 s73, s73, 4096\n"
      "s_mov_b64 exec, 1\n"
      "buffer_store_dword v2, off, s[60:63], s73 sc1\n"
      "s_mov_b64 exec, -1\n"
      "s_add_u32 s73, s73, 128\n"
      "s_mov_b64 exec, 1\n"
      "buffer_store_dword v2, off, s[60:63], s73 sc1\n"
      "s_mov_b64 exec, -1\n"
      "s_add_u32 m0, s27, 4096\n"
      "s_setprio 0\n"
      "buffer_load_dwordx4 v8, s[28:31], s72 offen lds\n"
      "s_setprio 1\n"
      "s_setprio 0\n"
      "buffer_load_dwordx4 v9, s[28:31], s72 offen offset:1024 lds\n"
      "s_setprio 1\n"
      "s_setprio 0\n"
      "buffer_load_dwordx4 v10, s[28:31], s72 offen offset:2048 lds\n"
      "s_setprio 1\n"
      "s_add_u32 m0, s27, 4096\n"
      "s_setprio 0\n"
      "buffer_load_dwordx4 v11, s[28:31], s72 offen offset:3072 lds\n"
      "s_setprio 1\n"
      "s_add_u32 s72, s72, 128\n"
      "s_cmp_eq_u32 s72, 1280\n"
      "s_cselect_b32 s72, 0, s72\n"
      "s_add_u32 m0, s27, 8192\n"
      "s_setprio 0\n"
      "buffer_load_dwordx4 v8, s[28:31], s72 offen lds\n"
      "s_setprio 1\n"
      "s_setprio 0\n"
      "buffer_load_dwordx4 v9, s[28:31], s72 offen offset:1024 lds\n"
      "s_setprio 1\n"
      "s_setprio 0\n"
      "buffer_load_dwordx4 v10, s[28:31], s72 offen offset:2048 lds\n"
      "s_setprio 1\n"
      "s_add_u32 m0, s27, 8192\n"
      "s_setprio 0\n"
      "buffer_load_dwordx4 v11, s[28:31], s72 offen offset:3072 lds\n"
      "s_setprio 1\n"
      "s_add_u32 s72, s72, 128\n"
      "s_cmp_eq_u32 s72, 1280\n"
      "s_cselect_b32 s72, 0, s72\n"
      "v_and_b32 v6, 15, v0\n"
      "v_bfe_u32 v7, v0, 4, 1\n"
      "v_lshlrev_b32 v7, 10, v7\n"
      "v_lshl_add_u32 v6, v6, 4, v7\n"
      "v_lshrrev_b32 v7, 5, v0\n"
      "v_lshl_add_u32 v1, v7, 8, v6\n"
      "s_mov_b64 s[44:45], s[60:61]\n"
      "s_cmp_lt_u32 s54, 6\n"
      "s_cselect_b32 s46, 23068672, 0\n"
      "s_mul_i32 s10, s64, 3072\n"
      "s_add_u32 s10, s10, 40960\n"
      "ds_read_b128 v[96:99], v12\n"
      "ds_read_b128 v[100:103], v13\n"
      "ds_read_b128 v[104:107], v14\n"
      "ds_read_b128 v[108:111], v15\n"
      "s_waitcnt lgkmcnt(0)\n"
      "s_add_u32 m0, s27, 0\n"
      "s_setprio 0\n"
      "buffer_load_dwordx4 v8, s[28:31], s72 offen lds\n"
      "s_setprio 1\n"
      "s_add_u32 m0, s27, 0\n"
      "s_setprio 0\n"
      "buffer_load_dwordx4 v9, s[28:31], s72 offen offset:1024 lds\n"
      "s_setprio 1\n"
      "s_add_u32 m0, s27, 0\n"
      "s_setprio 0\n"
      "buffer_load_dwordx4 v10, s[28:31], s72 offen offset:2048 lds\n"
      "s_setprio 1\n"
      "s_add_u32 m0, s27, 0\n"
      "s_setprio 0\n"
      "buffer_load_dwordx4 v11, s[28:31], s72 offen offset:3072 lds\n"
      "s_setprio 1\n"
      "s_add_u32 s72, s72, 128\n"
      "s_cmp_eq_u32 s72, 1280\n"
      "s_cselect_b32 s72, 0, s72\n"
      "s_waitcnt vmcnt(8)\n"
      "ds_read_b128 v[112:115], v12 offset:4096\n"
      "ds_read_b128 v[116:119], v13 offset:4096\n"
      "ds_read_b128 v[120:123], v14 offset:4096\n"
      "ds_read_b128 v[124:127], v15 offset:4096\n"
      "s_waitcnt lgkmcnt(0)\n"
      "s_add_u32 m0, s27, 4096\n"
      "s_setprio 0\n"
      "buffer_load_dwordx4 v8, s[28:31], s72 offen lds\n"
      "s_setprio 1\n"
      "s_add_u32 m0, s27, 4096\n"
      "s_setprio 0\n"
      "buffer_load_dwordx4 v9, s[28:31], s72 offen offset:1024 lds\n"
      "s_setprio 1\n"
      "s_add_u32 m0, s27, 4096\n"
      "s_setprio 0\n"
      "buffer_load_dwordx4 v10, s[28:31], s72 offen offset:2048 lds\n"
      "s_setprio 1\n"
      "s_add_u32 m0, s27, 4096\n"
      "s_setprio 0\n"
      "buffer_load_dwordx4 v11, s[28:31], s72 offen offset:3072 lds\n"
      "s_setprio 1\n"
      "s_add_u32 s72, s72, 128\n"
      "s_cmp_eq_u32 s72, 1280\n"
      "s_cselect_b32 s72, 0, s72\n"
      "s_waitcnt vmcnt(8)\n"
      "ds_read_b128 v[128:131], v12 offset:8192\n"
      "ds_read_b128 v[132:135], v13 offset:8192\n"
      "ds_read_b128 v[136:139], v14 offset:8192\n"
      "ds_read_b128 v[140:143], v15 offset:8192\n"
      "s_waitcnt lgkmcnt(0)\n"
      "s_add_u32 m0, s27, 8192\n"
      "s_setprio 0\n"
      "buffer_load_dwordx4 v8, s[28:31], s72 offen lds\n"
      "s_setprio 1\n"
      "s_add_u32 m0, s27, 8192\n"
      "s_setprio 0\n"
      "buffer_load_dwordx4 v9, s[28:31], s72 offen offset:1024 lds\n"
      "s_setprio 1\n"
      "s_add_u32 m0, s27, 8192\n"
      "s_setprio 0\n"
      "buffer_load_dwordx4 v10, s[28:31], s72 offen offset:2048 lds\n"
      "s_setprio 1\n"
      "s_add_u32 m0, s27, 8192\n"
      "s_setprio 0\n"
      "buffer_load_dwordx4 v11, s[28:31], s72 offen offset:3072 lds\n"
      "s_setprio 1\n"
      "s_add_u32 s72, s72, 128\n"
      "s_cmp_eq_u32 s72, 1280\n"
      "s_cselect_b32 s72, 0, s72\n"
      "s_cmp_lg_u32 s64, 3\n"
      "s_cbranch_scc1 Lai_25\n"
      "s_lshl_b32 s33, s66, 7\n"
      "s_add_u32 s33, s33, 4096\n"
      "v_min_u32 v32, 19, v0\n"
      "v_lshlrev_b32 v32, 2, v32\n"
      "buffer_load_dword v33, v32, s[60:63], s33 offen sc1\n"
      "Lai_25:\n"
      "s_cmp_lg_u32 s64, 3\n"
      "s_cbranch_scc1 Lacd_28\n"
      "s_waitcnt vmcnt(0)\n"
      "s_mov_b32 s96, 20000\n"
      "Lac_27:\n"
      "v_cmp_ne_u32 vcc, s26, v33\n"
      "s_cmp_eq_u64 vcc, 0\n"
      "s_cbranch_scc1 Lacd_28\n"
      "s_sub_u32 s96, s96, 1\n"
      "s_cmp_eq_u32 s96, 0\n"
      "s_cbranch_scc1 Lacd_28\n"
      "s_sleep 4\n"
      "buffer_load_dword v33, v32, s[60:63], s33 offen sc1\n"
      "s_waitcnt vmcnt(0)\n"
      "s_branch Lac_27\n"
      "Lacd_28:\n"
      "s_barrier\n"
      "s_add_u32 m0, s53, 16384\n"
      "s_add_u32 s97, s34, s35\n"
      "s_setprio 0\n"
      "buffer_load_dwordx4 v5, s[60:63], s97 offen sc1 lds\n"
      "s_setprio 1\n"
      "s_add_u32 s34, s34, 8192\n"
      "s_cmp_eq_u32 s34, 81920\n"
      "s_cselect_b32 s34, 0, s34\n"
      "s_add_u32 m0, s53, 24576\n"
      "s_add_u32 s97, s34, s35\n"
      "s_setprio 0\n"
      "buffer_load_dwordx4 v5, s[60:63], s97 offen sc1 lds\n"
      "s_setprio 1\n"
      "s_add_u32 s34, s34, 8192\n"
      "s_cmp_eq_u32 s34, 81920\n"
      "s_cselect_b32 s34, 0, s34\n"
      "s_add_u32 m0, s53, 32768\n"
      "s_add_u32 s97, s34, s35\n"
      "s_setprio 0\n"
      "buffer_load_dwordx4 v5, s[60:63], s97 offen sc1 lds\n"
      "s_setprio 1\n"
      "s_add_u32 s34, s34, 8192\n"
      "s_cmp_eq_u32 s34, 81920\n"
      "s_cselect_b32 s34, 0, s34\n"
      "s_waitcnt vmcnt(0)\n"
      "s_barrier\n"
      "ds_read_b128 v[32:35], v5 offset:16384\n"
      "ds_read_b128 v[36:39], v5 offset:17408\n"
      "ds_read_b128 v[40:43], v5 offset:18432\n"
      "ds_read_b128 v[44:47], v5 offset:19456\n"
      "ds_read_b128 v[48:51], v5 offset:20480\n"
      "ds_read_b128 v[52:55], v5 offset:21504\n"
      "ds_read_b128 v[56:59], v5 offset:22528\n"
      "ds_read_b128 v[60:63], v5 offset:23552\n"
      "s_waitcnt lgkmcnt(7)\n"
      "v_mfma_f32_32x32x16_bf16 v[64:79], v[32:35], v[96:99], 0\n"
      "s_add_u32 m0, s53, 40960\n"
      "s_add_u32 s97, s34, s35\n"
      "s_setprio 0\n"
      "buffer_load_dwordx4 v5, s[60:63], s97 offen sc1 lds\n"
      "s_setprio 1\n"
      "s_add_u32 s34, s34, 8192\n"
      "s_cmp_eq_u32 s34, 81920\n"
      "s_cselect_b32 s34, 0, s34\n"
      "s_waitcnt lgkmcnt(6)\n"
      "v_mfma_f32_32x32x16_bf16 v[80:95], v[36:39], v[96:99], 0\n"
      "ds_read_b128 v[32:35], v5 offset:24576\n"
      "ds_read_b128 v[36:39], v5 offset:25600\n"
      "s_add_u32 m0, s53, 49152\n"
      "s_add_u32 s97, s34, s35\n"
      "s_setprio 0\n"
      "buffer_load_dwordx4 v5, s[60:63], s97 offen sc1 lds\n"
      "s_setprio 1\n"
      "s_add_u32 s34, s34, 8192\n"
      "s_cmp_eq_u32 s34, 81920\n"
      "s_cselect_b32 s34, 0, s34\n"
      "s_waitcnt lgkmcnt(7)\n"
      "v_mfma_f32_32x32x16_bf16 v[64:79], v[40:43], v[100:103], v[64:79]\n"
      "s_add_u32 m0, s53, 57344\n"
      "s_add_u32 s97, s34, s35\n"
      "s_setprio 0\n"
      "buffer_load_dwordx4 v5, s[60:63], s97 offen sc1 lds\n"
      "s_setprio 1\n"
      "s_add_u32 s34, s34, 8192\n"
      "s_cmp_eq_u32 s34, 81920\n"
      "s_cselect_b32 s34, 0, s34\n"
      "s_waitcnt lgkmcnt(6)\n"
      "v_mfma_f32_32x32x16_bf16 v[80:95], v[44:47], v[100:103], v[80:95]\n"
      "ds_read_b128 v[40:43], v5 offset:26624\n"
      "ds_read_b128 v[44:47], v5 offset:27648\n"
      "s_add_u32 m0, s53, 0\n"
      "s_add_u32 s97, s34, s35\n"
      "s_setprio 0\n"
      "buffer_load_dwordx4 v5, s[60:63], s97 offen sc1 lds\n"
      "s_setprio 1\n"
      "s_add_u32 s34, s34, 8192\n"
      "s_cmp_eq_u32 s34, 81920\n"
      "s_cselect_b32 s34, 0, s34\n"
      "ds_read_b128 v[144:147], v12\n"
      "ds_read_b128 v[148:151], v13\n"
      "ds_read_b128 v[152:155], v14\n"
      "ds_read_b128 v[156:159], v15\n"
      "s_waitcnt lgkmcnt(11)\n"
      "v_mfma_f32_32x32x16_bf16 v[64:79], v[48:51], v[104:107], v[64:79]\n"
      "s_add_u32 m0, s53, 8192\n"
      "s_add_u32 s97, s34, s35\n"
      "s_setprio 0\n"
      "buffer_load_dwordx4 v5, s[60:63], s97 offen sc1 lds\n"
      "s_setprio 1\n"
      "s_add_u32 s34, s34, 8192\n"
      "s_cmp_eq_u32 s34, 81920\n"
      "s_cselect_b32 s34, 0, s34\n"
      "s_waitcnt lgkmcnt(10)\n"
      "v_mfma_f32_32x32x16_bf16 v[80:95], v[52:55], v[104:107], v[80:95]\n"
      "ds_read_b128 v[48:51], v5 offset:28672\n"
      "ds_read_b128 v[52:55], v5 offset:29696\n"
      "s_waitcnt lgkmcnt(2)\n"
      "v_mfma_f32_32x32x16_bf16 v[64:79], v[56:59], v[108:111], v[64:79]\n"
      "s_add_u32 m0, s27, 0\n"
      "s_setprio 0\n"
      "buffer_load_dwordx4 v8, s[28:31], s72 offen lds\n"
      "s_setprio 1\n"
      "v_mfma_f32_32x32x16_bf16 v[80:95], v[60:63], v[108:111], v[80:95]\n"
      "ds_read_b128 v[56:59], v5 offset:30720\n"
      "ds_read_b128 v[60:63], v5 offset:31744\n"
      "s_add_u32 m0, s27, 0\n"
      "s_setprio 0\n"
      "buffer_load_dwordx4 v9, s[28:31], s72 offen offset:1024 lds\n"
      "s_setprio 1\n"
      "v_mfma_f32_32x32x16_bf16 v[64:79], v[32:35], v[112:115], v[64:79]\n"
      "s_add_u32 m0, s27, 0\n"
      "s_setprio 0\n"
      "buffer_load_dwordx4 v10, s[28:31], s72 offen offset:2048 lds\n"
      "s_setprio 1\n"
      "v_mfma_f32_32x32x16_bf16 v[80:95], v[36:39], v[112:115], v[80:95]\n"
      "ds_read_b128 v[32:35], v5 offset:32768\n"
      "ds_read_b128 v[36:39], v5 offset:33792\n"
      "s_add_u32 m0, s27, 0\n"
      "s_setprio 0\n"
      "buffer_load_dwordx4 v11, s[28:31], s72 offen offset:3072 lds\n"
      "s_setprio 1\n"
      "s_add_u32 s72, s72, 128\n"
      "s_cmp_eq_u32 s72, 1280\n"
      "s_cselect_b32 s72, 0, s72\n"
      "v_mfma_f32_32x32x16_bf16 v[64:79], v[40:43], v[116:119], v[64:79]\n"
      "v_mfma_f32_32x32x16_bf16 v[80:95], v[44:47], v[116:119], v[80:95]\n"
      "ds_read_b128 v[40:43], v5 offset:34816\n"
      "ds_read_b128 v[44:47], v5 offset:35840\n"
      "ds_read_b128 v[160:163], v12 offset:4096\n"
      "ds_read_b128 v[164:167], v13 offset:4096\n"
      "ds_read_b128 v[168:171], v14 offset:4096\n"
      "ds_read_b128 v[172:175], v15 offset:4096\n"
      "s_waitcnt lgkmcnt(11)\n"
      "v_mfma_f32_32x32x16_bf16 v[64:79], v[48:51], v[120:123], v[64:79]\n"
      "s_waitcnt lgkmcnt(10)\n"
      "v_mfma_f32_32x32x16_bf16 v[80:95], v[52:55], v[120:123], v[80:95]\n"
      "ds_read_b128 v[48:51], v5 offset:36864\n"
      "ds_read_b128 v[52:55], v5 offset:37888\n"
      "s_waitcnt lgkmcnt(2)\n"
      "v_mfma_f32_32x32x16_bf16 v[64:79], v[56:59], v[124:127], v[64:79]\n"
      "s_add_u32 m0, s27, 4096\n"
      "s_setprio 0\n"
      "buffer_load_dwordx4 v8, s[28:31], s72 offen lds\n"
      "s_setprio 1\n"
      "v_mfma_f32_32x32x16_bf16 v[80:95], v[60:63], v[124:127], v[80:95]\n"
      "ds_read_b128 v[56:59], v5 offset:38912\n"
      "ds_read_b128 v[60:63], v5 offset:39936\n"
      "s_add_u32 m0, s27, 4096\n"
      "s_setprio 0\n"
      "buffer_load_dwordx4 v9, s[28:31], s72 offen offset:1024 lds\n"
      "s_setprio 1\n"
      "v_mfma_f32_32x32x16_bf16 v[64:79], v[32:35], v[128:131], v[64:79]\n"
      "s_add_u32 m0, s27, 4096\n"
      "s_setprio 0\n"
      "buffer_load_dwordx4 v10, s[28:31], s72 offen offset:2048 lds\n"
      "s_setprio 1\n"
      "v_mfma_f32_32x32x16_bf16 v[80:95], v[36:39], v[128:131], v[80:95]\n"
      "s_add_u32 m0, s27, 4096\n"
      "s_setprio 0\n"
      "buffer_load_dwordx4 v11, s[28:31], s72 offen offset:3072 lds\n"
      "s_setprio 1\n"
      "s_add_u32 s72, s72, 128\n"
      "s_cmp_eq_u32 s72, 1280\n"
      "s_cselect_b32 s72, 0, s72\n"
      "v_mfma_f32_32x32x16_bf16 v[64:79], v[40:43], v[132:135], v[64:79]\n"
      "v_mfma_f32_32x32x16_bf16 v[80:95], v[44:47], v[132:135], v[80:95]\n"
      "ds_read_b128 v[176:179], v12 offset:8192\n"
      "ds_read_b128 v[180:183], v13 offset:8192\n"
      "ds_read_b128 v[184:187], v14 offset:8192\n"
      "ds_read_b128 v[188:191], v15 offset:8192\n"
      "s_waitcnt vmcnt(10)\n"
      "s_barrier\n"
      "ds_read_b128 v[32:35], v5 offset:40960\n"
      "ds_read_b128 v[36:39], v5 offset:41984\n"
      "ds_read_b128 v[40:43], v5 offset:43008\n"
      "ds_read_b128 v[44:47], v5 offset:44032\n"
      "s_waitcnt lgkmcnt(11)\n"
      "v_mfma_f32_32x32x16_bf16 v[64:79], v[48:51], v[136:139], v[64:79]\n"
      "s_add_u32 m0, s53, 16384\n"
      "s_add_u32 s97, s34, s35\n"
      "s_setprio 0\n"
      "buffer_load_dwordx4 v5, s[60:63], s97 offen sc1 lds\n"
      "s_setprio 1\n"
      "s_add_u32 s34, s34, 8192\n"
      "s_cmp_eq_u32 s34, 81920\n"
      "s_cselect_b32 s34, 0, s34\n"
      "s_waitcnt lgkmcnt(10)\n"
      "v_mfma_f32_32x32x16_bf16 v[80:95], v[52:55], v[136:139], v[80:95]\n"
      "ds_read_b128 v[48:51], v5 offset:45056\n"
      "ds_read_b128 v[52:55], v5 offset:46080\n"
      "s_add_u32 m0, s53, 24576\n"
      "s_add_u32 s97, s34, s35\n"
      "s_setprio 0\n"
      "buffer_load_dwordx4 v5, s[60:63], s97 offen sc1 lds\n"
      "s_setprio 1\n"
      "s_add_u32 s34, s34, 8192\n"
      "s_cmp_eq_u32 s34, 81920\n"
      "s_cselect_b32 s34, 0, s34\n"
      "s_waitcnt lgkmcnt(6)\n"
      "v_mfma_f32_32x32x16_bf16 v[64:79], v[56:59], v[140:143], v[64:79]\n"
      "s_add_u32 m0, s27, 8192\n"
      "s_setprio 0\n"
      "buffer_load_dwordx4 v8, s[28:31], s72 offen lds\n"
      "s_setprio 1\n"
      "v_mfma_f32_32x32x16_bf16 v[80:95], v[60:63], v[140:143], v[80:95]\n"
      "ds_read_b128 v[56:59], v5 offset:47104\n"
      "ds_read_b128 v[60:63], v5 offset:48128\n"
      "s_add_u32 m0, s27, 8192\n"
      "s_setprio 0\n"
      "buffer_load_dwordx4 v9, s[28:31], s72 offen offset:1024 lds\n"
      "s_setprio 1\n"
      "s_waitcnt lgkmcnt(7)\n"
      "v_mfma_f32_32x32x16_bf16 v[64:79], v[32:35], v[144:147], v[64:79]\n"
      "s_add_u32 m0, s27, 8192\n"
      "s_setprio 0\n"
      "buffer_load_dwordx4 v10, s[28:31], s72 offen offset:2048 lds\n"
      "s_setprio 1\n"
      "s_waitcnt lgkmcnt(6)\n"
      "v_mfma_f32_32x32x16_bf16 v[80:95], v[36:39], v[144:147], v[80:95]\n"
      "ds_read_b128 v[32:35], v5 offset:49152\n"
      "ds_read_b128 v[36:39], v5 offset:50176\n"
      "s_add_u32 m0, s27, 8192\n"
      "s_setprio 0\n"
      "buffer_load_dwordx4 v11, s[28:31], s72 offen offset:3072 lds\n"
      "s_setprio 1\n"
      "s_add_u32 s72, s72, 128\n"
      "s_cmp_eq_u32 s72, 1280\n"
      "s_cselect_b32 s72, 0, s72\n"
      "s_waitcnt lgkmcnt(7)\n"
      "v_mfma_f32_32x32x16_bf16 v[64:79], v[40:43], v[148:151], v[64:79]\n"
      "s_waitcnt lgkmcnt(6)\n"
      "v_mfma_f32_32x32x16_bf16 v[80:95], v[44:47], v[148:151], v[80:95]\n"
      "ds_read_b128 v[40:43], v5 offset:51200\n"
      "ds_read_b128 v[44:47], v5 offset:52224\n"
      "s_waitcnt vmcnt(10)\n"
      "ds_read_b128 v[192:195], v12\n"
      "ds_read_b128 v[196:199], v13\n"
      "ds_read_b128 v[200:203], v14\n"
      "ds_read_b128 v[204:207], v15\n"
      "s_waitcnt lgkmcnt(11)\n"
      "v_mfma_f32_32x32x16_bf16 v[64:79], v[48:51], v[152:155], v[64:79]\n"
      "s_waitcnt lgkmcnt(10)\n"
      "v_mfma_f32_32x32x16_bf16 v[80:95], v[52:55], v[152:155], v[80:95]\n"
      "ds_read_b128 v[48:51], v5 offset:53248\n"
      "ds_read_b128 v[52:55], v5 offset:54272\n"
      "s_waitcnt lgkmcnt(2)\n"
      "v_mfma_f32_32x32x16_bf16 v[64:79], v[56:59], v[156:159], v[64:79]\n"
      "s_add_u32 m0, s27, 0\n"
      "s_setprio 0\n"
      "buffer_load_dwordx4 v8, s[28:31], s72 offen lds\n"
      "s_setprio 1\n"
      "v_mfma_f32_32x32x16_bf16 v[80:95], v[60:63], v[156:159], v[80:95]\n"
      "ds_read_b128 v[56:59], v5 offset:55296\n"
      "ds_read_b128 v[60:63], v5 offset:56320\n"
      "s_add_u32 m0, s27, 0\n"
      "s_setprio 0\n"
      "buffer_load_dwordx4 v9, s[28:31], s72 offen offset:1024 lds\n"
      "s_setprio 1\n"
      "v_mfma_f32_32x32x16_bf16 v[64:79], v[32:35], v[160:163], v[64:79]\n"
      "s_add_u32 m0, s27, 0\n"
      "s_setprio 0\n"
      "buffer_load_dwordx4 v10, s[28:31], s72 offen offset:2048 lds\n"
      "s_setprio 1\n"
      "v_mfma_f32_32x32x16_bf16 v[80:95], v[36:39], v[160:163], v[80:95]\n"
      "ds_read_b128 v[32:35], v5 offset:57344\n"
      "ds_read_b128 v[36:39], v5 offset:58368\n"
      "s_add_u32 m0, s27, 0\n"
      "s_setprio 0\n"
      "buffer_load_dwordx4 v11, s[28:31], s72 offen offset:3072 lds\n"
      "s_setprio 1\n"
      "s_add_u32 s72, s72, 128\n"
      "s_cmp_eq_u32 s72, 1280\n"
      "s_cselect_b32 s72, 0, s72\n"
      "v_mfma_f32_32x32x16_bf16 v[64:79], v[40:43], v[164:167], v[64:79]\n"
      "v_mfma_f32_32x32x16_bf16 v[80:95], v[44:47], v[164:167], v[80:95]\n"
      "ds_read_b128 v[40:43], v5 offset:59392\n"
      "ds_read_b128 v[44:47], v5 offset:60416\n"
      "s_waitcnt vmcnt(10)\n"
      "ds_read_b128 v[208:211], v12 offset:4096\n"
      "ds_read_b128 v[212:215], v13 offset:4096\n"
      "ds_read_b128 v[216:219], v14 offset:4096\n"
      "ds_read_b128 v[220:223], v15 offset:4096\n"
      "s_cmp_lg_u32 s64, 7\n"
      "s_cbranch_scc1 Lai_29\n"
      "s_mul_i32 s33, s71, 80\n"
      "s_lshr_b32 s33, s33, 6\n"
      "s_mul_i32 s33, s33, 296\n"
      "s_add_u32 s33, s33, 8192\n"
      "v_min_u32 v128, 36, v0\n"
      "v_lshlrev_b32 v128, 4, v128\n"
      "buffer_load_dwordx4 v[130:133], v128, s[60:63], s33 offen sc1\n"
      "Lai_29:\n"
      "s_waitcnt lgkmcnt(11)\n"
      "v_mfma_f32_32x32x16_bf16 v[64:79], v[48:51], v[168:171], v[64:79]\n"
      "s_waitcnt lgkmcnt(10)\n"
      "v_mfma_f32_32x32x16_bf16 v[80:95], v[52:55], v[168:171], v[80:95]\n"
      "ds_read_b128 v[48:51], v5 offset:61440\n"
      "ds_read_b128 v[52:55], v5 offset:62464\n"
      "s_waitcnt lgkmcnt(2)\n"
      "v_mfma_f32_32x32x16_bf16 v[64:79], v[56:59], v[172:175], v[64:79]\n"
      "v_mfma_f32_32x32x16_bf16 v[80:95], v[60:63], v[172:175], v[80:95]\n"
      "ds_read_b128 v[56:59], v5 offset:63488\n"
      "ds_read_b128 v[60:63], v5 offset:64512\n"
      "v_mfma_f32_32x32x16_bf16 v[64:79], v[32:35], v[176:179], v[64:79]\n"
      "v_mfma_f32_32x32x16_bf16 v[80:95], v[36:39], v[176:179], v[80:95]\n"
      "v_mfma_f32_32x32x16_bf16 v[64:79], v[40:43], v[180:183], v[64:79]\n"
      "v_mfma_f32_32x32x16_bf16 v[80:95], v[44:47], v[180:183], v[80:95]\n"
      "s_waitcnt vmcnt(4)\n"
      "ds_read_b128 v[224:227], v12 offset:8192\n"
      "ds_read_b128 v[228:231], v13 offset:8192\n"
      "ds_read_b128 v[232:235], v14 offset:8192\n"
      "ds_read_b128 v[236:239], v15 offset:8192\n"
      "s_cmp_lg_u32 s64, 7\n"
      "s_cbranch_scc1 Lacd_32\n"
      "s_waitcnt vmcnt(0)\n"
      "s_mov_b32 s96, 20000\n"
      "Lac_31:\n"
      "v_xor_b32 v130, s26, v130\n"
      "v_xor_b32 v131, s26, v131\n"
      "v_xor_b32 v132, s26, v132\n"
      "v_xor_b32 v133, s26, v133\n"
      "v_or3_b32 v129, v130, v131, v132\n"
      "v_or_b32 v129, v129, v133\n"
      "v_cmp_ne_u32 vcc, 0, v129\n"
      "s_cmp_eq_u64 vcc, 0\n"
      "s_cbranch_scc1 Lacd_32\n"
      "s_sub_u32 s96, s96, 1\n"
      "s_cmp_eq_u32 s96, 0\n"
      "s_cbranch_scc1 Lacd_32\n"
      "s_sleep 4\n"
      "buffer_load_dwordx4 v[130:133], v128, s[60:63], s33 offen sc1\n"
      "s_waitcnt vmcnt(0)\n"
      "s_branch Lac_31\n"
      "Lacd_32:\n"
      "s_barrier\n"
      "ds_read_b128 v[32:35], v5 offset:0\n"
      "ds_read_b128 v[36:39], v5 offset:1024\n"
      "ds_read_b128 v[40:43], v5 offset:2048\n"
      "ds_read_b128 v[44:47], v5 offset:3072\n"
      "s_waitcnt lgkmcnt(11)\n"
      "v_mfma_f32_32x32x16_bf16 v[64:79], v[48:51], v[184:187], v[64:79]\n"
      "s_waitcnt lgkmcnt(10)\n"
      "v_mfma_f32_32x32x16_bf16 v[80:95], v[52:55], v[184:187], v[80:95]\n"
      "ds_read_b128 v[48:51], v5 offset:4096\n"
      "ds_read_b128 v[52:55], v5 offset:5120\n"
      "s_waitcnt lgkmcnt(6)\n"
      "v_mfma_f32_32x32x16_bf16 v[64:79], v[56:59], v[188:191], v[64:79]\n"
      "v_mfma_f32_32x32x16_bf16 v[80:95], v[60:63], v[188:191], v[80:95]\n"
      "ds_read_b128 v[56:59], v5 offset:6144\n"
      "ds_read_b128 v[60:63], v5 offset:7168\n"
      "s_waitcnt lgkmcnt(7)\n"
      "v_mfma_f32_32x32x16_bf16 v[64:79], v[32:35], v[192:195], v[64:79]\n"
      "s_add_u32 s97, s79, 0\n"
      "s_setprio 0\n"
      "buffer_load_dwordx4 v[128:131], v5, s[60:63], s97 offen sc1\n"
      "s_setprio 1\n"
      "s_add_u32 s97, s79, 0\n"
      "s_setprio 0\n"
      "buffer_load_dwordx4 v[132:135], v5, s[60:63], s97 offen offset:1024 sc1\n"
      "s_setprio 1\n"
      "s_waitcnt lgkmcnt(6)\n"
      "v_mfma_f32_32x32x16_bf16 v[80:95], v[36:39], v[192:195], v[80:95]\n"
      "ds_read_b128 v[32:35], v5 offset:8192\n"
      "ds_read_b128 v[36:39], v5 offset:9216\n"
      "s_add_u32 s97, s79, 0\n"
      "s_setprio 0\n"
      "buffer_load_dwordx4 v[136:139], v5, s[60:63], s97 offen offset:2048 sc1\n"
      "s_setprio 1\n"
      "s_add_u32 s97, s79, 0\n"
      "s_setprio 0\n"
      "buffer_load_dwordx4 v[140:143], v5, s[60:63], s97 offen offset:3072 sc1\n"
      "s_setprio 1\n"
      "s_waitcnt lgkmcnt(7)\n"
      "v_mfma_f32_32x32x16_bf16 v[64:79], v[40:43], v[196:199], v[64:79]\n"
      "s_add_u32 s97, s79, 4096\n"
      "s_setprio 0\n"
      "buffer_load_dwordx4 v[144:147], v5, s[60:63], s97 offen sc1\n"
      "s_setprio 1\n"
      "s_add_u32 s97, s79, 40960\n"
      "s_setprio 0\n"
      "buffer_load_dwordx4 v[148:151], v5, s[60:63], s97 offen sc1\n"
      "s_setprio 1\n"
      "s_waitcnt lgkmcnt(6)\n"
      "v_mfma_f32_32x32x16_bf16 v[80:95], v[44:47], v[196:199], v[80:95]\n"
      "ds_read_b128 v[40:43], v5 offset:10240\n"
      "ds_read_b128 v[44:47], v5 offset:11264\n"
      "s_add_u32 s97, s79, 40960\n"
      "s_setprio 0\n"
      "buffer_load_dwordx4 v[152:155], v5, s[60:63], s97 offen offset:1024 sc1\n"
      "s_setprio 1\n"
      "s_add_u32 s97, s79, 40960\n"
      "s_setprio 0\n"
      "buffer_load_dwordx4 v[156:159], v5, s[60:63], s97 offen offset:2048 sc1\n"
      "s_setprio 1\n"
      "s_waitcnt vmcnt(8)\n"
      "ds_read_b128 v[240:243], v12\n"
      "ds_read_b128 v[244:247], v13\n"
      "ds_read_b128 v[248:251], v14\n"
      "ds_read_b128 v[252:255], v15\n"
      "s_waitcnt lgkmcnt(11)\n"
      "v_mfma_f32_32x32x16_bf16 v[64:79], v[48:51], v[200:203], v[64:79]\n"
      "s_add_u32 s97, s79, 40960\n"
      "s_setprio 0\n"
      "buffer_load_dwordx4 v[160:163], v5, s[60:63], s97 offen offset:3072 sc1\n"
      "s_setprio 1\n"
      "s_add_u32 s97, s79, 45056\n"
      "s_setprio 0\n"
      "buffer_load_dwordx4 v[164:167], v5, s[60:63], s97 offen sc1\n"
      "s_setprio 1\n"
      "s_waitcnt lgkmcnt(10)\n"
      "v_mfma_f32_32x32x16_bf16 v[80:95], v[52:55], v[200:203], v[80:95]\n"
      "ds_read_b128 v[48:51], v5 offset:12288\n"
      "ds_read_b128 v[52:55], v5 offset:13312\n"
      "s_add_u32 s97, s79, 81920\n"
      "s_setprio 0\n"
      "buffer_load_dwordx4 v[168:171], v5, s[60:63], s97 offen sc1\n"
      "s_setprio 1\n"
      "s_waitcnt lgkmcnt(2)\n"
      "v_mfma_f32_32x32x16_bf16 v[64:79], v[56:59], v[204:207], v[64:79]\n"
      "s_add_u32 s97, s79, 81920\n"
      "s_setprio 0\n"
      "buffer_load_dwordx4 v[172:175], v5, s[60:63], s97 offen offset:1024 sc1\n"
      "s_setprio 1\n"
      "v_mfma_f32_32x32x16_bf16 v[80:95], v[60:63], v[204:207], v[80:95]\n"
      "ds_read_b128 v[56:59], v5 offset:14336\n"
      "ds_read_b128 v[60:63], v5 offset:15360\n"
      "s_add_u32 s97, s79, 81920\n"
      "s_setprio 0\n"
      "buffer_load_dwordx4 v[176:179], v5, s[60:63], s97 offen offset:2048 sc1\n"
      "s_setprio 1\n"
      "v_mfma_f32_32x32x16_bf16 v[64:79], v[32:35], v[208:211], v[64:79]\n"
      "s_add_u32 s97, s79, 81920\n"
      "s_setprio 0\n"
      "buffer_load_dwordx4 v[180:183], v5, s[60:63], s97 offen offset:3072 sc1\n"
      "s_setprio 1\n"
      "s_add_u32 s97, s79, 86016\n"
      "s_setprio 0\n"
      "buffer_load_dwordx4 v[184:187], v5, s[60:63], s97 offen sc1\n"
      "s_setprio 1\n"
      "v_mfma_f32_32x32x16_bf16 v[80:95], v[36:39], v[208:211], v[80:95]\n"
      "ds_read_b128 v[32:35], v5 offset:16384\n"
      "ds_read_b128 v[36:39], v5 offset:17408\n"
      "s_add_u32 m0, s27, 0\n"
      "s_add_u32 s97, s79, 163840\n"
      "s_setprio 0\n"
      "buffer_load_dwordx4 v5, s[60:63], s97 offen sc1 lds\n"
      "s_setprio 1\n"
      "v_mfma_f32_32x32x16_bf16 v[64:79], v[40:43], v[212:215], v[64:79]\n"
      "s_add_u32 m0, s27, 0\n"
      "s_add_u32 s97, s79, 163840\n"
      "s_setprio 0\n"
      "buffer_load_dwordx4 v5, s[60:63], s97 offen offset:1024 sc1 lds\n"
      "s_setprio 1\n"
      "v_mfma_f32_32x32x16_bf16 v[80:95], v[44:47], v[212:215], v[80:95]\n"
      "ds_read_b128 v[40:43], v5 offset:18432\n"
      "ds_read_b128 v[44:47], v5 offset:19456\n"
      "s_add_u32 m0, s27, 0\n"
      "s_add_u32 s97, s79, 163840\n"
      "s_setprio 0\n"
      "buffer_load_dwordx4 v5, s[60:63], s97 offen offset:2048 sc1 lds\n"
      "s_setprio 1\n"
      "s_waitcnt lgkmcnt(7)\n"
      "v_mfma_f32_32x32x16_bf16 v[64:79], v[48:51], v[216:219], v[64:79]\n"
      "s_add_u32 m0, s27, 0\n"
      "s_add_u32 s97, s79, 163840\n"
      "s_setprio 0\n"
      "buffer_load_dwordx4 v5, s[60:63], s97 offen offset:3072 sc1 lds\n"
      "s_setprio 1\n"
      "s_waitcnt lgkmcnt(6)\n"
      "v_mfma_f32_32x32x16_bf16 v[80:95], v[52:55], v[216:219], v[80:95]\n"
      "ds_read_b128 v[48:51], v5 offset:20480\n"
      "ds_read_b128 v[52:55], v5 offset:21504\n"
      "s_add_u32 m0, s27, 4096\n"
      "s_add_u32 s97, s79, 167936\n"
      "s_setprio 0\n"
      "buffer_load_dwordx4 v5, s[60:63], s97 offen sc1 lds\n"
      "s_setprio 1\n"
      "s_waitcnt lgkmcnt(7)\n"
      "v_mfma_f32_32x32x16_bf16 v[64:79], v[56:59], v[220:223], v[64:79]\n"
      "s_waitcnt lgkmcnt(6)\n"
      "v_mfma_f32_32x32x16_bf16 v[80:95], v[60:63], v[220:223], v[80:95]\n"
      "ds_read_b128 v[56:59], v5 offset:22528\n"
      "ds_read_b128 v[60:63], v5 offset:23552\n"
      "s_waitcnt lgkmcnt(7)\n"
      "v_mfma_f32_32x32x16_bf16 v[64:79], v[32:35], v[224:227], v[64:79]\n"
      "s_waitcnt lgkmcnt(6)\n"
      "v_mfma_f32_32x32x16_bf16 v[80:95], v[36:39], v[224:227], v[80:95]\n"
      "ds_read_b128 v[32:35], v5 offset:24576\n"
      "ds_read_b128 v[36:39], v5 offset:25600\n"
      "s_waitcnt lgkmcnt(7)\n"
      "v_mfma_f32_32x32x16_bf16 v[64:79], v[40:43], v[228:231], v[64:79]\n"
      "s_waitcnt lgkmcnt(6)\n"
      "v_mfma_f32_32x32x16_bf16 v[80:95], v[44:47], v[228:231], v[80:95]\n"
      "ds_read_b128 v[40:43], v5 offset:26624\n"
      "ds_read_b128 v[44:47], v5 offset:27648\n"
      "s_waitcnt lgkmcnt(7)\n"
      "v_mfma_f32_32x32x16_bf16 v[64:79], v[48:51], v[232:235], v[64:79]\n"
      "s_waitcnt lgkmcnt(6)\n"
      "v_mfma_f32_32x32x16_bf16 v[80:95], v[52:55], v[232:235], v[80:95]\n"
      "ds_read_b128 v[48:51], v5 offset:28672\n"
      "ds_read_b128 v[52:55], v5 offset:29696\n"
      "s_waitcnt lgkmcnt(7)\n"
      "v_mfma_f32_32x32x16_bf16 v[64:79], v[56:59], v[236:239], v[64:79]\n"
      "s_waitcnt lgkmcnt(6)\n"
      "v_mfma_f32_32x32x16_bf16 v[80:95], v[60:63], v[236:239], v[80:95]\n"
      "ds_read_b128 v[56:59], v5 offset:30720\n"
      "ds_read_b128 v[60:63], v5 offset:31744\n"
      "s_waitcnt lgkmcnt(7)\n"
      "v_mfma_f32_32x32x16_bf16 v[64:79], v[32:35], v[240:243], v[64:79]\n"
      "s_waitcnt lgkmcnt(6)\n"
      "v_mfma_f32_32x32x16_bf16 v[80:95], v[36:39], v[240:243], v[80:95]\n"
      "s_waitcnt lgkmcnt(5)\n"
      "v_mfma_f32_32x32x16_bf16 v[64:79], v[40:43], v[244:247], v[64:79]\n"
      "s_waitcnt lgkmcnt(4)\n"
      "v_mfma_f32_32x32x16_bf16 v[80:95], v[44:47], v[244:247], v[80:95]\n"
      "s_waitcnt lgkmcnt(3)\n"
      "v_mfma_f32_32x32x16_bf16 v[64:79], v[48:51], v[248:251], v[64:79]\n"
      "s_waitcnt lgkmcnt(2)\n"
      "v_mfma_f32_32x32x16_bf16 v[80:95], v[52:55], v[248:251], v[80:95]\n"
      "s_waitcnt lgkmcnt(1)\n"
      "v_mfma_f32_32x32x16_bf16 v[64:79], v[56:59], v[252:255], v[64:79]\n"
      "s_waitcnt lgkmcnt(0)\n"
      "v_mfma_f32_32x32x16_bf16 v[80:95], v[60:63], v[252:255], v[80:95]\n"
      "s_add_u32 s97, s79, 122880\n"
      "s_setprio 0\n"
      "buffer_load_dwordx4 v[32:35], v5, s[60:63], s97 offen sc1\n"
      "s_setprio 1\n"
      "s_add_u32 s97, s79, 122880\n"
      "s_setprio 0\n"
      "buffer_load_dwordx4 v[36:39], v5, s[60:63], s97 offen offset:1024 sc1\n"
      "s_setprio 1\n"
      "s_add_u32 s97, s79, 122880\n"
      "s_setprio 0\n"
      "buffer_load_dwordx4 v[40:43], v5, s[60:63], s97 offen offset:2048 sc1\n"
      "s_setprio 1\n"
      "s_add_u32 s97, s79, 122880\n"
      "s_setprio 0\n"
      "buffer_load_dwordx4 v[44:47], v5, s[60:63], s97 offen offset:3072 sc1\n"
      "s_setprio 1\n"
      "s_add_u32 s97, s79, 126976\n"
      "s_setprio 0\n"
      "buffer_load_dwordx4 v[48:51], v5, s[60:63], s97 offen sc1\n"
      "s_setprio 1\n"
      "s_cmp_lg_u32 s64, 3\n"
      "s_cbranch_scc1 Lai_33\n"
      "s_lshl_b32 s33, s65, 2\n"
      "s_add_u32 s33, s33, s70\n"
      "s_lshl_b32 s33, s33, 8\n"
      "s_add_u32 s33, s33, 12288\n"
      "v_min_u32 v12, 53, v0\n"
      "v_lshlrev_b32 v12, 2, v12\n"
      "buffer_load_dword v13, v12, s[60:63], s33 offen sc1\n"
      "Lai_33:\n"
      "s_waitcnt vmcnt(0)\n"
      "s_branch L_afterg0\n"
      "L_end2:\n"
      :
      : "s"(__builtin_amdgcn_kernarg_segment_ptr()), "s"(blockIdx.x), "s"(wave), "s"((unsigned)(size_t)smem)
      : "memory", "vcc", "v255", "s5", "s6", "s7", "s8", "s9", "s10", "s11", "s12", "s13", "s14", "s15", "s16", "s17", "s18", "s19", "s20", "s21", "s22", "s23", "s24", "s25", "s26", "s27", "s28", "s29", "s30", "s31", "s33", "s34", "s35", "s36", "s37", "s38", "s39", "s40", "s41", "s42", "s43", "s44", "s45", "s46", "s47", "s48", "s49", "s50", "s51", "s52", "s53", "s54", "s55", "s56", "s57", "s58", "s59", "s60", "s61", "s62", "s63", "s64", "s65", "s66", "s67", "s68", "s69", "s70", "s71", "s72", "s73", "s74", "s75", "s76", "s77", "s78", "s79", "s80", "s81", "s82", "s83", "s84", "s85", "s86", "s87", "s88", "s89", "s90", "s91", "s92", "s93", "s94", "s95", "s96", "s97", "s98", "s99");
}

extern "C" void kernel_launch(void* const* d_in, const int* in_sizes, int n_in, void* d_out, int out_size, void* d_ws,
                              size_t ws_size, hipStream_t stream) {
  hedgehog_fused<<<256, 512, 0, stream>>>(d_in[0], d_in[1], d_in[2], d_in[3], d_in[5], d_in[6], d_in[7], d_out, d_ws);
}
```
